```python
import torch

def _softmax(x, axis=-1):
    return torch.softmax(x, dim=axis)
def _gelu(x):
    return torch.nn.functional.gelu(x, approximate="tanh")
def _silu(x):
    return torch.nn.functional.silu(x)
def _sigmoid(x):
    return torch.sigmoid(x)
def _layernorm(x, axis=-1, eps=1e-5):
    m = x.mean(dim=axis, keepdim=True)
    v = x.var(dim=axis, keepdim=True, unbiased=False)
    return (x - m) / torch.sqrt(v + eps)
def _scatter_add(tgt, idx, src, axis=0):
    return tgt.clone().index_add_(axis, idx, src)
def _segment_sum(src, seg, n):
    out = torch.zeros((n,) + src.shape[1:], dtype=src.dtype)
    return out.index_add_(0, seg, src)
def _pad(x, pw):
    flat = tuple(v for pair in reversed(pw) for v in pair)
    return torch.nn.functional.pad(x, flat)

kernel_name = 'concat_heads__gqa_block_cached__sliding_block_cached__decode'

def setup_inputs():

    _w = (lambda a: (lambda t: t.to(torch.bfloat16) if t.is_floating_point() else t)(torch.as_tensor(a)))
    def _A_setup():
        B, Sq, D = 1, 1, 1024
        Hq, Hkv, Sk, Dh = 32, 4, 65536, 128
        assert Hq % Hkv == 0, (Hq, Hkv)
        return {
            "x": _w((lambda *s: torch.randn(*s, dtype=torch.bfloat16) * 0.4)(B, Sq, D)),
            "k_cache": _w((lambda *s: torch.randn(*s, dtype=torch.bfloat16) * 3.2)(B, Hkv, Sk, Dh)),
            "v_cache": _w((lambda *s: torch.randn(*s, dtype=torch.bfloat16) * 0.8)(B, Hkv, Sk, Dh)),
            "wq": _w((lambda *s: torch.randn(*s, dtype=torch.bfloat16) * 3.2)(D, Hq * Dh) * (D ** -0.5)),
            "wo": _w((lambda *s: torch.randn(*s, dtype=torch.bfloat16) * 0.8)(Hq * Dh, D) * ((Hq * Dh) ** -0.5)),
        }
    _A_in = _A_setup()

    def _B_setup():
        B, Sq, D = 1, 1, 1024
        Hq, Hkv, Sk, Dh = 32, 4, 65536, 128
        assert Hq % Hkv == 0, (Hq, Hkv)
        return {
            "x": _w((lambda *s: torch.randn(*s, dtype=torch.bfloat16) * 0.2)(B, Sq, D)),
            "k_cache": _w((lambda *s: torch.randn(*s, dtype=torch.bfloat16) * 3.2)(B, Hkv, Sk, Dh)),
            "v_cache": _w((lambda *s: torch.randn(*s, dtype=torch.bfloat16) * 1.6)(B, Hkv, Sk, Dh)),
            "wq": _w((lambda *s: torch.randn(*s, dtype=torch.bfloat16) * 3.2)(D, Hq * Dh) * (D ** -0.5)),
            "wo": _w((lambda *s: torch.randn(*s, dtype=torch.bfloat16) * 1.6)(Hq * Dh, D) * ((Hq * Dh) ** -0.5)),
        }
    _B_in = _B_setup()

    return {
        "A_x": _A_in["x"],
        "A_k_cache": _A_in["k_cache"],
        "A_v_cache": _A_in["v_cache"],
        "A_wq": _A_in["wq"],
        "A_wo": _A_in["wo"],
        "B_x": _B_in["x"],
        "B_k_cache": _B_in["k_cache"],
        "B_v_cache": _B_in["v_cache"],
        "B_wq": _B_in["wq"],
        "B_wo": _B_in["wo"],
    }

def reference(A_x, A_k_cache, A_v_cache, A_wq, A_wo, B_x, B_k_cache, B_v_cache, B_wq, B_wo):
    def _A_ref(x, k_cache, v_cache, wq, wo):
        B, Sq, D = x.shape
        Hq, Dh = 32, 128
        Hkv = k_cache.shape[1]
        rep = Hq // Hkv
        q = (lambda x, *d: x.permute(*d))((x @ wq).reshape(B, Sq, Hq, Dh), 0, 2, 1, 3)
        k = (lambda x, n, axis: torch.repeat_interleave(x, n, dim=axis))(k_cache, rep, axis=1)
        v = (lambda x, n, axis: torch.repeat_interleave(x, n, dim=axis))(v_cache, rep, axis=1)
        scores = torch.einsum("bhqd,bhkd->bhqk", q, k) * (Dh ** -0.5)
        p = _softmax(scores, axis=-1)
        o = torch.einsum("bhqk,bhkd->bhqd", p, v)
        return (lambda x, *d: x.permute(*d))(o, 0, 2, 1, 3).reshape(B, Sq, Hq * Dh) @ wo

    def _B_ref(x, k_cache, v_cache, wq, wo):
        B, Sq, D = x.shape
        Hq, Dh = 32, 128
        Hkv, Sk = k_cache.shape[1], k_cache.shape[2]
        W = min(4096, max(256, Sk // 4))
        rep = Hq // Hkv
        q = (lambda x, *d: x.permute(*d))((x @ wq).reshape(B, Sq, Hq, Dh), 0, 2, 1, 3)
        k = (lambda x, n, axis: torch.repeat_interleave(x, n, dim=axis))(k_cache[:, :, Sk - W:, :], rep, axis=1)
        v = (lambda x, n, axis: torch.repeat_interleave(x, n, dim=axis))(v_cache[:, :, Sk - W:, :], rep, axis=1)
        scores = torch.einsum("bhqd,bhkd->bhqk", q, k) * (Dh ** -0.5)
        p = _softmax(scores, axis=-1)
        o = torch.einsum("bhqk,bhkd->bhqd", p, v)
        return (lambda x, *d: x.permute(*d))(o, 0, 2, 1, 3).reshape(B, Sq, Hq * Dh) @ wo

    a = _A_ref(A_x, A_k_cache, A_v_cache, A_wq, A_wo)
    b = _B_ref(B_x, B_k_cache, B_v_cache, B_wq, B_wo)
    return torch.concatenate([a, b], axis=-1)
```

```cpp
#include <hip/hip_runtime.h>
#include <hip/hip_bf16.h>
#include <stdint.h>

#define GAS __attribute__((address_space(1)))
#define LAS __attribute__((address_space(3)))

namespace fa {
typedef unsigned v4u __attribute__((ext_vector_type(4)));
typedef unsigned v2u __attribute__((ext_vector_type(2)));
typedef float f32x4 __attribute__((ext_vector_type(4)));
typedef float f32x2 __attribute__((ext_vector_type(2)));
typedef short s16x4 __attribute__((ext_vector_type(4)));
typedef __bf16 bf16x8 __attribute__((ext_vector_type(8)));
typedef __bf16 bf16x2 __attribute__((ext_vector_type(2)));

constexpr int SK = 65536, WINB = 4096;
constexpr int NWG = 256, NCONS = 4, NTHR = (NCONS + 1) * 64;
constexpr int NSLOT = 16, SLOTB = 8192;
#define FA_INFL 3
constexpr int INFL = FA_INFL;
constexpr int NTILE = 34, TB0 = 20, TB1 = 24;
static_assert(TB1 == TB0 + NCONS && TB0 % NCONS == 0 && TB1 + NCONS < NTILE, "B tiles: two consecutive tiles of consumer 0");
constexpr int F_KV = 8, F_WO = F_KV + 2 * NTILE, NFILL = F_WO + 8;
constexpr float C2 = 0.08838834764831845f * 1.4426950408889634f;

constexpr int MRG_H = 528, MRG_W = 8 * MRG_H, QSTR = 272;
constexpr int L_MRG = NSLOT * SLOTB, L_ML = L_MRG + NCONS * MRG_W, L_Q = L_ML + 256, L_X = L_Q + 2 * 8 * QSTR,
              L_HS = L_X + 1024, L_CTL = L_HS + 512, L_END = L_CTL + 64;
static_assert(L_END <= 160 * 1024 && (L_X % 16) == 0 && (L_HS % 16) == 0 && (L_CTL % 16) == 0, "lds map");
constexpr int WS_QP = 4096, WS_ML = WS_QP + 2 * 4 * 2048 * 16, WS_PART = WS_ML + 2 * 4 * 8 * 64 * 16,
              WS_OP = WS_PART + 2 * 4 * 8 * 64 * 1024, WS_END = WS_OP + 2 * 128 * 8192;
constexpr int AUX_SC1 = 16, AUX_NT = 2;
constexpr int SPIN_BUDGET = 1 << 22, GL_COST = 16;

struct Params {
  const unsigned char *xA, *kA, *vA, *wqA, *woA, *xB, *kB, *vB, *wqB, *woB;
  unsigned* out;
  unsigned char* ws;
};

#define STAMP(i) do {} while (0)

#define CBAR() asm volatile("" ::: "memory")
template <int N> __device__ __forceinline__ void waitvm() { asm volatile("s_waitcnt vmcnt(%0)" ::"i"(N) : "memory"); }
__device__ __forceinline__ void waitlgkm() { asm volatile("s_waitcnt lgkmcnt(0)" ::: "memory"); }
__device__ __forceinline__ void fill8(const unsigned char* ubase, const unsigned (&vo)[8], unsigned dst) {
  asm volatile(
      "s_mov_b32 m0, %9\n\ts_nop 4\n\t"
      "global_load_lds_dwordx4 %0, %8 nt\n\t"
      "global_load_lds_dwordx4 %1, %8 offset:1024 nt\n\t"
      "global_load_lds_dwordx4 %2, %8 offset:2048 nt\n\t"
      "global_load_lds_dwordx4 %3, %8 offset:3072 nt\n\t"
      "s_or_b32 m0, %9, 0x1000\n\ts_nop 0\n\t"
      "global_load_lds_dwordx4 %4, %8 nt\n\t"
      "global_load_lds_dwordx4 %5, %8 offset:1024 nt\n\t"
      "global_load_lds_dwordx4 %6, %8 offset:2048 nt\n\t"
      "global_load_lds_dwordx4 %7, %8 offset:3072 nt"
      ::"v"(vo[0]), "v"(vo[1]), "v"(vo[2]), "v"(vo[3]), "v"(vo[4]), "v"(vo[5]), "v"(vo[6]), "v"(vo[7]), "s"(ubase), "s"(dst)
      : "memory", "scc");
}
__device__ __forceinline__ f32x4 mfma(v4u a, v4u b, f32x4 c) {
  return __builtin_amdgcn_mfma_f32_16x16x32_bf16(__builtin_bit_cast(bf16x8, a), __builtin_bit_cast(bf16x8, b), c, 0, 0, 0);
}
__device__ __forceinline__ v2u trread(LAS unsigned char* p) {
  return __builtin_bit_cast(v2u, __builtin_amdgcn_ds_read_tr16_b64_v4i16((LAS s16x4*)p));
}
__device__ __forceinline__ unsigned pack2(float a, float b) {
  f32x2 t = {a, b};
  return __builtin_bit_cast(unsigned, __builtin_convertvector(t, bf16x2));
}
__device__ __forceinline__ float u2f(unsigned x) { return __builtin_bit_cast(float, x); }
__device__ __forceinline__ unsigned f2u(float x) { return __builtin_bit_cast(unsigned, x); }
template <int CTRL> __device__ __forceinline__ float dpp(float x) {
  return u2f((unsigned)__builtin_amdgcn_mov_dpp((int)f2u(x), CTRL, 0xf, 0xf, true));
}
constexpr int XOR1 = 0xB1, XOR2 = 0x4E, ROR4 = 0x124, ROR8 = 0x128;
__device__ __forceinline__ float xrow_max(float x) {
  auto s = __builtin_amdgcn_permlane16_swap(f2u(x), f2u(x), false, false);
  x = fmaxf(u2f(s[0]), u2f(s[1]));
  auto t = __builtin_amdgcn_permlane32_swap(f2u(x), f2u(x), false, false);
  return fmaxf(u2f(t[0]), u2f(t[1]));
}
__device__ __forceinline__ float xrow_sum(float x) {
  auto s = __builtin_amdgcn_permlane16_swap(f2u(x), f2u(x), false, false);
  x = u2f(s[0]) + u2f(s[1]);
  auto t = __builtin_amdgcn_permlane32_swap(f2u(x), f2u(x), false, false);
  return u2f(t[0]) + u2f(t[1]);
}
__device__ __forceinline__ v4u ldsc1(__amdgpu_buffer_rsrc_t rs, int voff, int soff) { return __builtin_amdgcn_raw_buffer_load_b128(rs, voff, soff, AUX_SC1); }
__device__ __forceinline__ void stpair(__amdgpu_buffer_rsrc_t rs, int voff, int soff, float a, float b, unsigned t0, unsigned t1) {
  v4u g = {f2u(a), t0, f2u(b), t1};
  __builtin_amdgcn_raw_buffer_store_b128(g, rs, voff + soff, 0, AUX_SC1);
}
__device__ __forceinline__ float sel(bool c, float a, float b) {
  asm("" : "+v"(a));
  return c ? a : b;
}
__device__ __forceinline__ bool tagok(v4u g, unsigned t0, unsigned t1) { return g.y == t0 && g.w == t1; }

typedef volatile LAS unsigned* ctl_t;
__device__ __forceinline__ void wait_ge(ctl_t w, unsigned need, int& budget) {
#pragma unroll 1
  while ((unsigned)__builtin_amdgcn_readfirstlane(*w) < need && --budget > 0) __builtin_amdgcn_s_sleep(1);
  CBAR();
}
__device__ __forceinline__ void lds_arrive(ctl_t w, int lane) {
  waitlgkm();
  if (lane == 0) __hip_atomic_fetch_add((LAS unsigned*)w, 1u, __ATOMIC_RELAXED, __HIP_MEMORY_SCOPE_WORKGROUP);
  CBAR();
}

__device__ __forceinline__ void attn_tile(LAS unsigned char* L, ctl_t ctl, int wave, int lane, int fK, int nextProg, unsigned kofs, unsigned vofs,
                                          const v4u (&Qf)[4], float& m, float& l, f32x4 (&O)[8], int& budget) {
  LAS unsigned char* sK = L + (fK & (NSLOT - 1)) * SLOTB;
  LAS unsigned char* sV = L + ((fK + 1) & (NSLOT - 1)) * SLOTB;
  wait_ge(ctl + 4, fK + 1, budget);
  v4u Kf[2][4];
#pragma unroll
  for (int t = 0; t < 2; ++t)
#pragma unroll
    for (int s = 0; s < 4; ++s) Kf[t][s] = *(LAS v4u*)(sK + ((kofs ^ (s << 6)) + t * 4096));
  waitlgkm();
  ctl[wave] = fK + 1;
  CBAR();
  f32x4 S0 = {0.f, 0.f, 0.f, 0.f}, S1 = {0.f, 0.f, 0.f, 0.f};
#pragma unroll
  for (int s = 0; s < 4; ++s) {
    S0 = mfma(Kf[0][s], Qf[s], S0);
    S1 = mfma(Kf[1][s], Qf[s], S1);
  }
  float tm = fmaxf(fmaxf(fmaxf(S0.x, S0.y), fmaxf(S0.z, S0.w)), fmaxf(fmaxf(S1.x, S1.y), fmaxf(S1.z, S1.w)));
  tm = xrow_max(tm);
  const float mn = fmaxf(m, tm);
  if (__any(mn > m)) {
    const float al = __builtin_amdgcn_exp2f((m - mn) * C2);
    l *= al;
#pragma unroll
    for (int e = 0; e < 8; ++e) O[e] *= al;
    m = mn;
  }
  const float mb = -m * C2;
  float p[8];
#pragma unroll
  for (int i = 0; i < 4; ++i) {
    p[i] = __builtin_amdgcn_exp2f(fmaf(S0[i], C2, mb));
    p[4 + i] = __builtin_amdgcn_exp2f(fmaf(S1[i], C2, mb));
  }
  l += ((p[0] + p[1]) + (p[2] + p[3])) + ((p[4] + p[5]) + (p[6] + p[7]));
  const v4u Pf = {pack2(p[0], p[1]), pack2(p[2], p[3]), pack2(p[4], p[5]), pack2(p[6], p[7])};
  wait_ge(ctl + 4, fK + 2, budget);
  v4u Vf[8];
#pragma unroll
  for (int e = 0; e < 8; ++e) {
    LAS unsigned char* a = sV + (vofs ^ (e << 5));
    const v2u lo = trread(a), hi = trread(a + 4096);
    Vf[e] = v4u{lo.x, lo.y, hi.x, hi.y};
  }
  waitlgkm();
  ctl[wave] = nextProg;
  CBAR();
#pragma unroll
  for (int e = 0; e < 8; ++e) O[e] = mfma(Vf[e], Pf, O[e]);
}

__global__ void __launch_bounds__(NTHR, 2) fused(Params p) {
  __shared__ __attribute__((aligned(8192))) unsigned char smem[L_END];
  LAS unsigned char* L = (LAS unsigned char*)smem;
  ctl_t ctl = (ctl_t)(L + L_CTL);
  const int tid = threadIdx.x, lane = tid & 63, wave = __builtin_amdgcn_readfirstlane(tid >> 6);
  const int b = blockIdx.x, g = b >> 6, s = b & 63;
  const int cg = s >> 2, ksp = s & 3;
  const int br = s >> 5;
  int budget = SPIN_BUDGET;
  STAMP(0);

  if (wave == NCONS) {
    __builtin_amdgcn_s_setprio(3);
    const unsigned lbase = (unsigned)(uintptr_t)L;
    if (lane < 16) ctl[lane] = 0u;
    {
      const unsigned char* xs = (lane < 32) ? p.xA : p.xB;
      asm volatile("s_mov_b32 m0, %1\n\ts_nop 0\n\tglobal_load_lds_dwordx4 %0, off" ::"v"(xs + ksp * 512 + (lane & 31) * 16), "s"(lbase + L_X) : "memory");
    }
    unsigned vq[8], vk[8], vv[8], vw[8];
#pragma unroll
    for (int i = 0; i < 8; ++i) {
      vq[i] = (unsigned)(lane >> 3) * 8192u + ((unsigned)(((lane & 7) ^ (((lane >> 4) & 1) << 1)) << 4) ^ ((i & 1) << 6)) + i * 65536u - (i & 3) * 1024u;
      vk[i] = (unsigned)(lane >> 4) * 256u + ((unsigned)(((lane & 15) ^ (lane >> 4)) << 4) ^ ((i & 3) << 6)) + (i >> 2) * 4096u;
      vv[i] = (unsigned)(lane >> 4) * 256u + ((unsigned)(((lane & 15) ^ ((lane >> 4) << 1)) << 4) ^ ((i & 1) << 7)) + (i >> 2) * 4096u;
      vw[i] = (((unsigned)lane * 16u) ^ ((i >> 1) << 5)) + (i >> 2) * 4096u;
    }
    const size_t qoff = ((size_t)ksp * 256 * 4096 + g * 1024 + cg * 64) * 2;
    const size_t aoff = ((size_t)g * SK + s * 1024) * 256, boff = ((size_t)g * SK + (SK - WINB) + s * 64) * 256;
    const unsigned char* wo = (br ? p.woB : p.woA) + ((size_t)g * 1024 + (s & 31) * 32) * 2048;
    int pub = 0;
    auto publish = [&](int cnt) { pub = max(pub, cnt); ctl[4] = (unsigned)pub; };
    auto minprog = [&]() {
      const v4u pr = *(volatile LAS v4u*)(L + L_CTL);
      return (int)__builtin_amdgcn_readfirstlane(min(min(pr.x, pr.y), min(pr.z, pr.w)));
    };
    auto park = [&](int f, int need) {
      if (INFL > 5) { waitvm<40>(); publish(f - 5); if (minprog() > need) return; }
      if (INFL > 4) { waitvm<32>(); publish(f - 4); if (minprog() > need) return; }
      if (INFL > 3) { waitvm<24>(); publish(f - 3); if (minprog() > need) return; }
      if (INFL > 2) { waitvm<16>(); publish(f - 2); if (minprog() > need) return; }
      waitvm<8>(); publish(f - 1); if (minprog() > need) return;
      waitvm<0>(); publish(f);
#pragma unroll 1
      while (minprog() <= need && --budget > 0) __builtin_amdgcn_s_sleep(1);
    };
#pragma unroll
    for (int f = 0; f < F_KV; ++f) {
      fill8(((f < 4) ? p.wqA : p.wqB) + qoff + (size_t)(f & 3) * 64 * 8192, vq, lbase + f * SLOTB);
      if (f == INFL - 1) {
        waitlgkm();
        __builtin_amdgcn_s_barrier();
      }
      if (f >= INFL) {
        waitvm<8 * INFL>();
        publish(f + 1 - INFL);
      }
    }
    STAMP(2);
    const unsigned char *ka = p.kA + aoff, *va = p.vA + aoff;
    int mp = 0;
#pragma unroll 1
    for (int t = 0; t < NTILE; ++t) {
      const int f = F_KV + 2 * t;
      const bool isB = (t == TB0) || (t == TB1);
      const size_t ob = boff + (t == TB1 ? 8192 : 0);
      const unsigned char *kp = isB ? p.kB + ob : ka, *vp = isB ? p.vB + ob : va;
      if (mp <= f + 1 - NSLOT) park(f, f + 1 - NSLOT);
      CBAR();
      fill8(kp, vk, lbase + (f & (NSLOT - 1)) * SLOTB);
      waitvm<8 * INFL>();
      publish(f + 1 - INFL);
      fill8(vp, vv, lbase + ((f + 1) & (NSLOT - 1)) * SLOTB);
      const v4u pr = *(volatile LAS v4u*)(L + L_CTL);
      waitvm<8 * INFL>();
      publish(f + 2 - INFL);
      mp = (int)__builtin_amdgcn_readfirstlane(min(min(pr.x, pr.y), min(pr.z, pr.w)));
      if (!isB) { ka += 8192; va += 8192; }
    }
    STAMP(5);
#pragma unroll
    for (int j = 0; j < 8; ++j) {
      const int f = F_WO + j;
      if (mp <= f - NSLOT) { park(f, f - NSLOT); mp = f - NSLOT + 1; }
      CBAR();
      if (j == 2 || j == 4 || j == 6) {
#pragma unroll
        for (int i = 0; i < 8; ++i) vw[i] ^= 128u;
      }
      fill8(wo + j * 8192, vw, lbase + (f & (NSLOT - 1)) * SLOTB);
      waitvm<8 * INFL>();
      publish(f + 1 - INFL);
      if (j == INFL - 1) STAMP(6);
      if (j < 7) mp = max(mp, minprog());
    }
    if (INFL > 5) { waitvm<40>(); publish(NFILL - 5); }
    if (INFL > 4) { waitvm<32>(); publish(NFILL - 4); }
    if (INFL > 3) { waitvm<24>(); publish(NFILL - 3); }
    if (INFL > 2) { waitvm<16>(); publish(NFILL - 2); }
    waitvm<8>(); publish(NFILL - 1);
    waitvm<0>(); publish(NFILL);
    STAMP(7);
    return;
  }

  const __amdgpu_buffer_rsrc_t rs = __builtin_amdgcn_make_buffer_rsrc((void*)p.ws, 0, WS_END, 0x00020000);
  const unsigned long long E = __hip_atomic_load((GAS unsigned long long*)p.ws, __ATOMIC_RELAXED, __HIP_MEMORY_SCOPE_AGENT);
  const int n = lane & 15, c = lane >> 4, q4 = (lane >> 2) & 3, p4 = lane & 3;
  __builtin_amdgcn_s_barrier();
  CBAR();
  const unsigned long long tg = E * 0x9E3779B97F4A7C15ull + 0xD1B54A32D192ED03ull;
  const unsigned t0 = __builtin_amdgcn_readfirstlane((unsigned)tg), t1 = __builtin_amdgcn_readfirstlane((unsigned)(tg >> 32));

  {
    const unsigned wofs = (unsigned)((8 * c + q4) * 128 + ((wave ^ (((q4 >> 1) & 1) | ((c & 1) << 1))) << 5) + 8 * p4);
#pragma unroll
    for (int bb = 0; bb < 2; ++bb) {
      f32x4 acc = {0.f, 0.f, 0.f, 0.f};
#pragma unroll
      for (int f = 0; f < 4; ++f) {
        wait_ge(ctl + 4, 4 * bb + f + 1, budget);
#pragma unroll
        for (int h = 0; h < 2; ++h) {
          LAS unsigned char* a = L + (4 * bb + f) * SLOTB + h * 4096 + wofs;
          const v2u lo = trread(a), hi = trread(a + 512);
          const v4u xf = *(LAS v4u*)(L + L_X + bb * 512 + (2 * f + h) * 64 + c * 16);
          acc = mfma(v4u{lo.x, lo.y, hi.x, hi.y}, xf, acc);
        }
      }
      waitlgkm();
      ctl[wave] = bb == 0 ? 4u : (unsigned)(F_KV + 2 * wave);
      CBAR();
      if (n < 2) {
        stpair(rs, (2 * c + n) * 16, WS_QP + ((bb * 4 + ksp) * 2048 + 512 * g + 32 * cg + 8 * wave) * 16, sel(n, acc.z, acc.x), sel(n, acc.w, acc.y), t0, t1);
      }
      STAMP(1 + bb);
    }
  }
  const int qgo = WS_QP + (512 * g + 128 * wave) * 16;
  auto q_issue = [&](int bb, v4u (&G)[8]) {
    CBAR();
#pragma unroll
    for (int j = 0; j < 8; ++j) G[j] = ldsc1(rs, lane * 16 + (j >> 2) * 1024, qgo + (bb * 4 + (j & 3)) * 32768);
  };
  auto q_finish = [&](int bb, v4u (&G)[8]) {
#pragma unroll 1
    for (;;) {
      bool ok = true;
#pragma unroll
      for (int j = 0; j < 8; ++j) ok &= tagok(G[j], t0, t1);
      if (__all(ok) || (budget -= GL_COST) <= 0) break;
      q_issue(bb, G);
    }
#pragma unroll
    for (int hh = 0; hh < 2; ++hh) {
      const float a0 = (u2f(G[4 * hh].x) + u2f(G[4 * hh + 1].x)) + (u2f(G[4 * hh + 2].x) + u2f(G[4 * hh + 3].x));
      const float a1 = (u2f(G[4 * hh].z) + u2f(G[4 * hh + 1].z)) + (u2f(G[4 * hh + 2].z) + u2f(G[4 * hh + 3].z));
      *(LAS unsigned*)(L + L_Q + (bb * 8 + 2 * wave + hh) * QSTR + lane * 4) = pack2(a0, a1);
    }
    lds_arrive(ctl + 5 + bb, lane);
  };
  {
    v4u G[8];
    q_issue(0, G);
    q_finish(0, G);
  }
  wait_ge(ctl + 5, NCONS, budget);
  v4u Qf[4];
#pragma unroll
  for (int k = 0; k < 4; ++k) Qf[k] = *(LAS v4u*)(L + L_Q + (n & 7) * QSTR + (4 * k + c) * 16);
  STAMP(3);

  const unsigned kofs = (unsigned)(n * 256 + ((c ^ n) << 4));
  const unsigned vofs = (unsigned)((4 * c + q4) * 256 + ((((c & 1) << 2) | q4) << 5) + 8 * p4);
  float m = -INFINITY, l = 0.f;
  f32x4 O[8];
#pragma unroll
  for (int e = 0; e < 8; ++e) O[e] = f32x4{0.f, 0.f, 0.f, 0.f};
  auto run_a = [&](int t0_, int t1_) {
#pragma unroll 1
    for (int t = t0_; t < t1_; t += NCONS) {
      const int fK = F_KV + 2 * t;
      attn_tile(L, ctl, wave, lane, fK, (t + NCONS < NTILE) ? fK + 2 * NCONS : F_WO, kofs, vofs, Qf, m, l, O, budget);
    }
  };
  run_a(wave, 2 * NCONS);
  STAMP(4);
  {
    v4u G[8];
    q_issue(1, G);
    run_a(wave + 2 * NCONS, 3 * NCONS);
    q_finish(1, G);
  }
  run_a(wave + 3 * NCONS, wave == 0 ? TB0 : NTILE);
  if (wave == 0) {
    {
      float mB = -INFINITY, lB = 0.f;
      f32x4 OB[8];
#pragma unroll
      for (int e = 0; e < 8; ++e) OB[e] = f32x4{0.f, 0.f, 0.f, 0.f};
      wait_ge(ctl + 6, NCONS, budget);
      v4u QB[4];
#pragma unroll
      for (int k = 0; k < 4; ++k) QB[k] = *(LAS v4u*)(L + L_Q + (8 + (n & 7)) * QSTR + (4 * k + c) * 16);
#pragma unroll 1
      for (int t = TB0; t <= TB1; t += NCONS) attn_tile(L, ctl, wave, lane, F_KV + 2 * t, F_KV + 2 * (t + NCONS), kofs, vofs, QB, mB, lB, OB, budget);
      const float lt = xrow_sum(lB);
      const int hrec = (4 + g) * 8 * 64 + s;
      const int o = (n & 7) * 65536 + (2 * c + (n >> 3)) * 16;
#pragma unroll
      for (int e = 0; e < 8; ++e) stpair(rs, o + e * 128, WS_PART + hrec * 1024, sel(n < 8, OB[e].x, OB[e].z), sel(n < 8, OB[e].y, OB[e].w), t0, t1);
      if (lane < 8) stpair(rs, lane * 1024, WS_ML + hrec * 16, mB * C2, lt, t0, t1);
    }
    run_a(TB1 + NCONS, NTILE);
  }
  STAMP(5);

  {
    const float lt = xrow_sum(l);
    LAS unsigned char* mw = L + L_MRG + wave * MRG_W + (n & 7) * MRG_H + c * 16 + (n >> 3) * 8;
#pragma unroll
    for (int e = 0; e < 8; ++e) *(LAS f32x2*)(mw + e * 64) = f32x2{sel(n < 8, O[e].x, O[e].z), sel(n < 8, O[e].y, O[e].w)};
    if (lane < 8) *(LAS f32x2*)(L + L_ML + (wave * 8 + lane) * 8) = f32x2{m * C2, lt};
    lds_arrive(ctl + 7, lane);
    wait_ge(ctl + 7, NCONS, budget);
    const int h = 2 * wave + (lane >> 5), j = lane & 31;
    f32x2 ml[4], a[4], bq[4];
#pragma unroll
    for (int w = 0; w < 4; ++w) {
      ml[w] = *(LAS f32x2*)(L + L_ML + (w * 8 + h) * 8);
      a[w] = *(LAS f32x2*)(L + L_MRG + w * MRG_W + h * MRG_H + j * 8);
      bq[w] = *(LAS f32x2*)(L + L_MRG + w * MRG_W + h * MRG_H + 256 + j * 8);
    }
    const float M = fmaxf(fmaxf(ml[0].x, ml[1].x), fmaxf(ml[2].x, ml[3].x));
    float Ls = 0.f;
    f32x2 ra = {0.f, 0.f}, rb = {0.f, 0.f};
#pragma unroll
    for (int w = 0; w < 4; ++w) {
      const float wt = __builtin_amdgcn_exp2f(ml[w].x - M);
      Ls = fmaf(wt, ml[w].y, Ls);
      ra += wt * a[w];
      rb += wt * bq[w];
    }
    const int hrec = (g * 8 + 2 * wave) * 64 + s;
    stpair(rs, (lane >> 5) * 65536 + j * 16, WS_PART + hrec * 1024, ra.x, ra.y, t0, t1);
    stpair(rs, (lane >> 5) * 65536 + 512 + j * 16, WS_PART + hrec * 1024, rb.x, rb.y, t0, t1);
    if (j == 0) stpair(rs, (lane >> 5) * 1024, WS_ML + hrec * 16, M, Ls, t0, t1);
  }
  STAMP(6);

  {
    const int hh = (s & 31) >> 2, qd = s & 3;
    const int hbase = ((br * 4 + g) * 8 + hh) * 64;
    const int od = WS_PART + (hbase + 16 * wave) * 1024 + qd * 256, om = WS_ML + hbase * 16;
    v4u D[4], mlg;
    auto h_issue = [&]() {
      CBAR();
      mlg = ldsc1(rs, lane * 16, om);
#pragma unroll
      for (int i = 0; i < 4; ++i) D[i] = ldsc1(rs, c * 1024 + n * 16, od + i * 4096);
    };
    h_issue();
    wait_ge(ctl + 4, NFILL, budget);
    STAMP(7);
    v4u Wf[16];
    {
      const unsigned h3 = (unsigned)(q4 | ((c & 1) << 2));
      LAS unsigned char* b0 = L + ((F_WO + 2 * c) & (NSLOT - 1)) * SLOTB + q4 * 2048 + wave * 512 + 8 * p4;
      LAS unsigned char* b1 = L + ((F_WO + 2 * c + 1) & (NSLOT - 1)) * SLOTB + q4 * 2048 + wave * 512 + 8 * p4;
#pragma unroll
      for (int k = 0; k < 16; ++k) {
        const unsigned o = (unsigned)((k >> 3) * 256) + (((unsigned)(k & 7) ^ h3) << 5);
        const v2u lo = trread(b0 + o), hi = trread(b1 + o);
        Wf[k] = v4u{lo.x, lo.y, hi.x, hi.y};
      }
    }
#pragma unroll 1
    for (;;) {
      bool ok = tagok(mlg, t0, t1);
#pragma unroll
      for (int i = 0; i < 4; ++i) ok &= tagok(D[i], t0, t1);
      if (__all(ok) || (budget -= GL_COST) <= 0) break;
      h_issue();
    }
    STAMP(8);
    float M = u2f(mlg.x);
    M = fmaxf(M, dpp<XOR1>(M)); M = fmaxf(M, dpp<XOR2>(M)); M = fmaxf(M, dpp<ROR4>(M)); M = fmaxf(M, dpp<ROR8>(M));
    M = xrow_max(M);
    const float wt = __builtin_amdgcn_exp2f(u2f(mlg.x) - M);
    float Ls = wt * u2f(mlg.z);
    Ls += dpp<XOR1>(Ls); Ls += dpp<XOR2>(Ls); Ls += dpp<ROR4>(Ls); Ls += dpp<ROR8>(Ls);
    Ls = xrow_sum(Ls);
    float a0 = 0.f, a1 = 0.f;
#pragma unroll
    for (int i = 0; i < 4; ++i) {
      const float wi = u2f((unsigned)__builtin_amdgcn_ds_bpermute((16 * wave + 4 * i + c) * 4, (int)f2u(wt)));
      a0 = fmaf(wi, u2f(D[i].x), a0);
      a1 = fmaf(wi, u2f(D[i].z), a1);
    }
    a0 = xrow_sum(a0);
    a1 = xrow_sum(a1);
    if (lane < 16) *(LAS f32x2*)(L + L_HS + wave * 128 + lane * 8) = f32x2{a0, a1};
    lds_arrive(ctl + 8, lane);
    wait_ge(ctl + 8, NCONS, budget);
    f32x4 o0 = {0.f, 0.f, 0.f, 0.f}, o1 = {0.f, 0.f, 0.f, 0.f};
#pragma unroll
    for (int w = 0; w < 4; ++w) {
      o0 += *(LAS f32x4*)(L + L_HS + w * 128 + c * 32);
      o1 += *(LAS f32x4*)(L + L_HS + w * 128 + c * 32 + 16);
    }
    const float inv = 1.0f / Ls;
    const v4u Of = {pack2(o0.x * inv, o0.y * inv), pack2(o0.z * inv, o0.w * inv), pack2(o1.x * inv, o1.y * inv), pack2(o1.z * inv, o1.w * inv)};
    f32x4 res = {0.f, 0.f, 0.f, 0.f};
#pragma unroll
    for (int k = 0; k < 16; ++k) {
      const f32x4 d = mfma(Wf[k], Of, f32x4{0.f, 0.f, 0.f, 0.f});
      res = (n == k) ? d : res;
    }
    const int oo = WS_OP + (br * 128 + g * 32 + (s & 31)) * 8192 + wave * 2048;
    stpair(rs, n * 64 + c * 16, oo, res.x, res.y, t0, t1);
    stpair(rs, n * 64 + c * 16 + 1024, oo, res.z, res.w, t0, t1);
  }
  STAMP(9);
  if (wave != 0) return;

  {
    const int n2 = ((b >> 3) & 1) | ((b & 7) << 1), k2 = (b >> 4) & 1, w2 = (b >> 5) & 3, br2 = b >> 7;
    const int of = WS_OP + br2 * 128 * 8192 + w2 * 2048 + k2 * 1024 + n2 * 64;
    v4u F[8];
#pragma unroll 1
    for (;;) {
      CBAR();
#pragma unroll
      for (int i = 0; i < 8; ++i) F[i] = ldsc1(rs, (lane >> 2) * 8192 + p4 * 16, of + i * 16 * 8192);
      bool ok = true;
#pragma unroll
      for (int i = 0; i < 8; ++i) ok &= tagok(F[i], t0, t1);
      if (__all(ok) || (budget -= GL_COST) <= 0) break;
    }
    float a0 = ((u2f(F[0].x) + u2f(F[1].x)) + (u2f(F[2].x) + u2f(F[3].x))) + ((u2f(F[4].x) + u2f(F[5].x)) + (u2f(F[6].x) + u2f(F[7].x)));
    float a1 = ((u2f(F[0].z) + u2f(F[1].z)) + (u2f(F[2].z) + u2f(F[3].z))) + ((u2f(F[4].z) + u2f(F[5].z)) + (u2f(F[6].z) + u2f(F[7].z)));
    a0 += dpp<ROR4>(a0); a0 += dpp<ROR8>(a0); a0 = xrow_sum(a0);
    a1 += dpp<ROR4>(a1); a1 += dpp<ROR8>(a1); a1 = xrow_sum(a1);
    if (lane < 4) p.out[(br2 * 1024 + 256 * w2 + 16 * n2 + 4 * lane + 2 * k2) >> 1] = pack2(a0, a1);
    if (b == 0 && lane == 0) __hip_atomic_store((GAS unsigned long long*)p.ws, tg, __ATOMIC_RELAXED, __HIP_MEMORY_SCOPE_AGENT);
  }
  STAMP(10);
}
}

extern "C" void kernel_launch(void* const* d_in, const int* in_sizes, int n_in, void* d_out, int out_size, void* d_ws, size_t ws_size,
                              hipStream_t stream) {
  (void)in_sizes; (void)n_in; (void)out_size; (void)ws_size;
  fa::Params p;
  p.xA = (const unsigned char*)d_in[0]; p.kA = (const unsigned char*)d_in[1]; p.vA = (const unsigned char*)d_in[2];
  p.wqA = (const unsigned char*)d_in[3]; p.woA = (const unsigned char*)d_in[4];
  p.xB = (const unsigned char*)d_in[5]; p.kB = (const unsigned char*)d_in[6]; p.vB = (const unsigned char*)d_in[7];
  p.wqB = (const unsigned char*)d_in[8]; p.woB = (const unsigned char*)d_in[9];
  p.out = (unsigned*)d_out;
  p.ws = (unsigned char*)d_ws;
  fa::fused<<<fa::NWG, fa::NTHR, 0, stream>>>(p);
}
```

```cpp
#include <hip/hip_runtime.h>
#include <hip/hip_bf16.h>
#include <stdint.h>

#define GAS __attribute__((address_space(1)))
#define LAS __attribute__((address_space(3)))

namespace fa {
typedef unsigned v4u __attribute__((ext_vector_type(4)));
typedef unsigned v2u __attribute__((ext_vector_type(2)));
typedef float f32x4 __attribute__((ext_vector_type(4)));
typedef float f32x2 __attribute__((ext_vector_type(2)));
typedef short s16x4 __attribute__((ext_vector_type(4)));
typedef __bf16 bf16x8 __attribute__((ext_vector_type(8)));
typedef __bf16 bf16x2 __attribute__((ext_vector_type(2)));

constexpr int SK = 65536, WINB = 4096;
constexpr int NWG = 256, NCONS = 4, NTHR = (NCONS + 1) * 64;
constexpr int NSLOT = 18, SLOTB = 8192;
constexpr int INFL = 2, WIDE = 3;
constexpr int NTA = 32;
constexpr int FKV = 8, FB = FKV + 2 * NTA, FWO = FB + 4, NFILL = FWO + 8;
constexpr int AT_QA = 8, AT_QB = 13, AT_Q = 20, AT_REC = NFILL - 5;
constexpr int R_BEACON = 4, R_STEER = 5;
constexpr float C2 = 0.08838834764831845f * 1.4426950408889634f;

constexpr int QSTR = 272, DSTR = 528;
constexpr int L_Q = NSLOT * SLOTB, L_X = L_Q + 2 * 8 * QSTR, L_O = L_X + 1024, L_W = L_O + 256, L_CS = L_W + 256, L_DEP = L_CS + 1024,
              L_CTL = L_DEP + 8 * DSTR, L_END = L_CTL + 48;
static_assert(L_END <= 160 * 1024 && (L_X % 16) == 0 && (L_O % 16) == 0 && (L_DEP % 16) == 0 && (L_CTL % 16) == 0, "lds map");
constexpr int C_FULL = L_CTL + 16, C_M = L_CTL + 20, C_W = L_CTL + 24, C_Q = L_CTL + 28, C_D = L_CTL + 32, C_O = L_CTL + 36, C_B = L_CTL + 40,
              ALL4 = 0x01010101;
constexpr int WS_QR = 4096, WS_QP = WS_QR + 2 * 256 * 512, WS_ML = WS_QP + 2 * 4 * 4096, WS_PART = WS_ML + 2 * 4 * 8 * 64 * 32,
              WS_OP = WS_PART + 2 * 4 * 8 * 64 * 256, WS_BC = WS_OP + 2 * 4 * 32 * 2048, WS_CT = WS_BC + 4 * 1024, WS_END = WS_CT + 64 * 128;
constexpr int SC1 = 16;
constexpr int SPIN_BUDGET = 1 << 22, POLL_BUDGET = 1 << 19;
constexpr int HINT_POLLS = 1 << 14;

struct Params {
  const unsigned char *wqA, *wqB, *xA, *xB, *kA, *vA, *kB, *vB, *woA, *woB;
  unsigned* out;
  unsigned char* ws;
};

template <int V> struct IC { static constexpr int value = V; };
#define CBAR() asm volatile("" ::: "memory")
template <int N> __device__ __forceinline__ void waitvm() { asm volatile("s_waitcnt vmcnt(%0)" ::"i"(N) : "memory"); }
__device__ __forceinline__ void fill8(const unsigned char* ubase, const unsigned (&vo)[8], unsigned dst) {
  asm volatile(
      "s_mov_b32 m0, %9\n\ts_nop 4\n\t"
      "global_load_lds_dwordx4 %0, %8 nt\n\tglobal_load_lds_dwordx4 %1, %8 offset:1024 nt\n\t"
      "global_load_lds_dwordx4 %2, %8 offset:2048 nt\n\tglobal_load_lds_dwordx4 %3, %8 offset:3072 nt\n\t"
      "s_or_b32 m0, %9, 0x1000\n\ts_nop 0\n\t"
      "global_load_lds_dwordx4 %4, %8 nt\n\tglobal_load_lds_dwordx4 %5, %8 offset:1024 nt\n\t"
      "global_load_lds_dwordx4 %6, %8 offset:2048 nt\n\tglobal_load_lds_dwordx4 %7, %8 offset:3072 nt"
      ::"v"(vo[0]), "v"(vo[1]), "v"(vo[2]), "v"(vo[3]), "v"(vo[4]), "v"(vo[5]), "v"(vo[6]), "v"(vo[7]), "s"(ubase), "s"(dst)
      : "memory", "scc");
}
__device__ __forceinline__ f32x4 mfma(v4u a, v4u b, f32x4 c) {
  return __builtin_amdgcn_mfma_f32_16x16x32_bf16(__builtin_bit_cast(bf16x8, a), __builtin_bit_cast(bf16x8, b), c, 0, 0, 0);
}
__device__ __forceinline__ v2u trread(LAS unsigned char* p) {
  return __builtin_bit_cast(v2u, __builtin_amdgcn_ds_read_tr16_b64_v4i16((LAS s16x4*)p));
}
__device__ __forceinline__ unsigned pack2(float a, float b) {
  f32x2 t = {a, b};
  return __builtin_bit_cast(unsigned, __builtin_convertvector(t, bf16x2));
}
__device__ __forceinline__ float u2f(unsigned x) { return __builtin_bit_cast(float, x); }
__device__ __forceinline__ unsigned f2u(float x) { return __builtin_bit_cast(unsigned, x); }
__device__ __forceinline__ float fmx(float a, float b) { return __builtin_elementwise_maximum(a, b); }
template <int CTRL> __device__ __forceinline__ unsigned dppu(unsigned x) { return (unsigned)__builtin_amdgcn_mov_dpp((int)x, CTRL, 0xf, 0xf, true); }
template <int CTRL> __device__ __forceinline__ float dpp(float x) { return u2f(dppu<CTRL>(f2u(x))); }
constexpr int HMIR = 0x141, XOR1 = 0xB1, XOR2 = 0x4E, ROR4 = 0x124, ROR8 = 0x128;
__device__ __forceinline__ float xrow_max(float x) {
  auto s = __builtin_amdgcn_permlane16_swap(f2u(x), f2u(x), false, false);
  x = fmx(u2f(s[0]), u2f(s[1]));
  auto t = __builtin_amdgcn_permlane32_swap(f2u(x), f2u(x), false, false);
  return fmx(u2f(t[0]), u2f(t[1]));
}
__device__ __forceinline__ float xrow_sum(float x) {
  auto s = __builtin_amdgcn_permlane16_swap(f2u(x), f2u(x), false, false);
  x = u2f(s[0]) + u2f(s[1]);
  auto t = __builtin_amdgcn_permlane32_swap(f2u(x), f2u(x), false, false);
  return u2f(t[0]) + u2f(t[1]);
}
__device__ __forceinline__ v4u ldsc1(__amdgpu_buffer_rsrc_t rs, int voff, int soff) { return __builtin_amdgcn_raw_buffer_load_b128(rs, voff, soff, SC1); }
__device__ __forceinline__ void stsc1(__amdgpu_buffer_rsrc_t rs, int off, v4u d) { __builtin_amdgcn_raw_buffer_store_b128(d, rs, off, 0, SC1); }
__device__ __forceinline__ void stpair(__amdgpu_buffer_rsrc_t rs, int off, unsigned a, unsigned b, unsigned t0, unsigned t1) { stsc1(rs, off, v4u{a, t0, b, t1}); }
__device__ __forceinline__ float sel(bool c, float a, float b) {
  asm("" : "+v"(a));
  return c ? a : b;
}
__device__ __forceinline__ bool tagok(v4u g, unsigned t0, unsigned t1) { return g.y == t0 && g.w == t1; }

__device__ __forceinline__ void wait_ge(unsigned a, int need, int& seen, int& budget) {
  unsigned v;
  seen = __builtin_amdgcn_readfirstlane(seen);
  budget = __builtin_amdgcn_readfirstlane(budget);
  need = __builtin_amdgcn_readfirstlane(need);
  asm volatile(
      "s_cmp_ge_i32 %0, %4\n\ts_cbranch_scc1 2f\n"
      "1:\n\tds_read_b32 %2, %3\n\ts_waitcnt lgkmcnt(0)\n\tv_readfirstlane_b32 %0, %2\n\t"
      "s_cmp_ge_i32 %0, %4\n\ts_cbranch_scc1 2f\n\t"
      "s_sub_i32 %1, %1, 1\n\ts_cmp_gt_i32 %1, 0\n\ts_sleep 1\n\ts_cbranch_scc1 1b\n"
      "2:"
      : "+s"(seen), "+s"(budget), "=&v"(v) : "v"(a), "s"(need) : "memory", "scc");
}

__global__ void __launch_bounds__(NTHR, 2) fused(Params p) {
  __shared__ __attribute__((aligned(8192))) unsigned char smem[L_END];
  LAS unsigned char* L = (LAS unsigned char*)smem;
  const unsigned lbase = (unsigned)(uintptr_t)L;
  volatile LAS unsigned* prog = (volatile LAS unsigned*)(L + L_CTL);
  const int tid = threadIdx.x, lane = tid & 63, wave = __builtin_amdgcn_readfirstlane(tid >> 6);
  const int b = blockIdx.x, g = b >> 6, s = b & 63;
  const int cg = (s & 7) | ((s >> 5) << 3), ksp = (s >> 3) & 3, br = s >> 5 ? 0 : 1, hh = s & 7;
  int budget = SPIN_BUDGET;

  if (!(wave < NCONS)) {
    __builtin_amdgcn_s_setprio(3);
    unsigned vk[8], vv[8], vw[8];
#pragma unroll
    for (int i = 0; i < 8; ++i)
      vw[i] = (unsigned)(lane >> 3) * 8192u + ((unsigned)(((lane & 7) ^ (((lane >> 4) & 1) << 1)) << 4) ^ ((i & 1) << 6)) + i * 65536u - (i & 3) * 1024u;
    const size_t qoff = ((size_t)ksp * 256 * 4096 + g * 1024 + cg * 64) * 2;
    int pub = 0;
    auto publish = [&](int cnt) { pub = max(pub, cnt); prog[4] = (unsigned)pub; };
#pragma unroll
    for (int f = 0; f < FKV; ++f) {
      fill8((f < 4 ? p.wqA : p.wqB) + qoff + (size_t)(f & 3) * 64 * 8192, vw, lbase + f * SLOTB);
      if (f == 0) {
        const unsigned char* xs = (lane < 32 ? p.xA : p.xB) + ksp * 512 + (lane & 31) * 16;
        asm volatile("s_mov_b32 m0, %1\n\ts_nop 0\n\tglobal_load_lds_dwordx4 %0, off" ::"v"(xs), "s"(lbase + L_X) : "memory");
        if (lane < 12) prog[lane] = 0u;
      }
      if (f == INFL - 1) {
        asm volatile("s_waitcnt lgkmcnt(0)" ::: "memory");
        __builtin_amdgcn_s_barrier();
      }
      if (f >= WIDE) { waitvm<8 * WIDE>(); publish(f + 1 - WIDE); }
    }
#pragma unroll
    for (int i = 0; i < 8; ++i) {
      vk[i] = (unsigned)(lane >> 4) * 256u + ((unsigned)(((lane & 15) ^ (lane >> 4)) << 4) ^ ((i & 3) << 6)) + (i >> 2) * 4096u;
      vv[i] = (unsigned)(lane >> 4) * 256u + ((unsigned)(((lane & 15) ^ ((lane >> 4) << 1)) << 4) ^ ((i & 1) << 7)) + (i >> 2) * 4096u;
    }
    auto minprog = [&]() {
      const v4u pr = *(volatile LAS v4u*)(L + L_CTL);
      return (int)__builtin_amdgcn_readfirstlane(min(min(pr.x, pr.y), min(pr.z, pr.w)));
    };
    auto park = [&](int f, int need) {
      static_assert(WIDE == 3 && INFL <= WIDE, "drain ladder");
      waitvm<16>(); publish(f - 2); if (minprog() > need) return;
      waitvm<8>(); publish(f - 1); if (minprog() > need) return;
      waitvm<0>(); publish(f);
#pragma unroll 1
      while (minprog() <= need && --budget > 0) __builtin_amdgcn_s_sleep(1);
    };
    int steer = 0;
    auto settle = [&](int f, int issued) {
      if (f < steer) { waitvm<8 * WIDE>(); publish(issued - WIDE); }
      else { waitvm<8 * INFL>(); publish(issued - INFL); if (f < -steer) __builtin_amdgcn_s_sleep(4); }
    };
    const size_t aoff = ((size_t)g * SK + s * 1024) * 256, boff = ((size_t)g * SK + (SK - WINB) + ((5 * s + 16 * g) & 63) * 32) * 256;
    const unsigned char *kp = p.kA + aoff, *vp = p.vA + aoff;
    int mp = 0;
    unsigned dst = lbase + FKV * SLOTB;
    auto tiles = [&](int f0, int f1, int adv) {
#pragma unroll 1
      for (int f = f0; f < f1; f += 2) {
        if (mp <= f + 1 - NSLOT) park(f, f + 1 - NSLOT);
        fill8(kp, vk, dst);
        settle(f, f + 1);
        fill8(vp, vv, dst + SLOTB);
        const v4u pr = *(volatile LAS v4u*)(L + L_CTL);
        kp += adv; vp += adv;
        dst = dst + 2 * SLOTB == lbase + NSLOT * SLOTB ? lbase : dst + 2 * SLOTB;
        const unsigned st = *(volatile LAS unsigned*)(L + C_B);
        settle(f, f + 2);
        mp = (int)__builtin_amdgcn_readfirstlane(min(min(pr.x, pr.y), min(pr.z, pr.w)));
        steer = (int)__builtin_amdgcn_readfirstlane(st);
      }
    };
    tiles(FKV, FB, 8192);
#pragma unroll
    for (int i = 0; i < 8; ++i)
      vw[i] = (unsigned)(2 * i + (lane >> 5)) * 2048u + ((((unsigned)lane & 31u) ^ ((unsigned)((2 * i + (lane >> 5)) & 7) << 1)) << 4) - (i & 3) * 1024u;
    kp = p.kB + boff; vp = p.vB + boff;
    tiles(FB, FWO, 2048 * 256);
    const unsigned char* wo = (br ? p.woB : p.woA) + ((size_t)(g * 8 + hh) * 128 * 1024 + ksp * 256) * 2;
#pragma unroll 1
    for (int f = FWO; f < NFILL; ++f) {
      if (mp <= f - NSLOT) { park(f, f - NSLOT); mp = f - NSLOT + 1; }
      fill8(wo, vw, dst);
      const v4u pr = *(volatile LAS v4u*)(L + L_CTL);
      wo += 32768;
      dst = dst + SLOTB == lbase + NSLOT * SLOTB ? lbase : dst + SLOTB;
      settle(f, f + 1);
      mp = max(mp, (int)__builtin_amdgcn_readfirstlane(min(min(pr.x, pr.y), min(pr.z, pr.w))));
    }
    waitvm<16>(); publish(NFILL - 2);
    waitvm<8>(); publish(NFILL - 1);
    waitvm<0>(); publish(NFILL);
    return;
  }

  const __amdgpu_buffer_rsrc_t rs = __builtin_amdgcn_make_buffer_rsrc((void*)p.ws, 0, WS_END, 0x00020000);
  unsigned long long E = __hip_atomic_load((GAS unsigned long long*)p.ws, __ATOMIC_RELAXED, __HIP_MEMORY_SCOPE_AGENT);
  const int n = lane & 15, c = lane >> 4, q4 = (lane >> 2) & 3, p4 = lane & 3;
  __builtin_amdgcn_s_barrier();
  asm volatile("" : "+v"(E)::"memory");
  const unsigned long long tg = E * 0x9E3779B97F4A7C15ull + 0xD1B54A32D192ED03ull;
  const unsigned t0 = __builtin_amdgcn_readfirstlane((unsigned)tg), t1 = __builtin_amdgcn_readfirstlane((unsigned)(tg >> 32));
  int full = 0, polls = POLL_BUDGET;
  auto wait_full = [&](int need) { wait_ge(lbase + C_FULL, need, full, budget); };
  auto wait_word = [&](int word, int need) { int seen = 0; wait_ge(lbase + word, need, seen, budget); };
  auto arrive = [&](int word) { CBAR(); *(volatile LAS unsigned char*)(L + word + wave) = 1; };
  auto counter = [&](int branch, int head) { return (GAS unsigned*)(p.ws + WS_CT + ((branch * 4 + g) * 8 + head) * 128); };

  if (wave == 2 && s < 16) {
    __hip_atomic_store(counter(s >> 3, s & 7), 0u, __ATOMIC_RELAXED, __HIP_MEMORY_SCOPE_AGENT);
    waitvm<0>();
  }
  const int qrec = (g * 16 + cg) * 4;
  {
    const unsigned wofs = (unsigned)((8 * c + q4) * 128 + ((wave ^ (((q4 >> 1) & 1) | ((c & 1) << 1))) << 5) + 8 * p4);
#pragma unroll 1
    for (int bb = 0; bb < 2; ++bb) {
      f32x4 acc = {0.f, 0.f, 0.f, 0.f};
#pragma unroll 1
      for (int f = 4 * bb; f < 4 * bb + 4; ++f) {
        wait_full(f + 1);
        LAS unsigned char* a = L + f * SLOTB + wofs;
        const v2u l0 = trread(a), h0 = trread(a + 512), l1 = trread(a + 4096), h1 = trread(a + 4096 + 512);
        const v4u x0 = *(LAS v4u*)(L + L_X + f * 128 + c * 16), x1 = *(LAS v4u*)(L + L_X + f * 128 + 64 + c * 16);
        acc = mfma(v4u{l0.x, l0.y, h0.x, h0.y}, x0, acc);
        acc = mfma(v4u{l1.x, l1.y, h1.x, h1.y}, x1, acc);
      }
      if (n < 2) stpair(rs, WS_QR + (bb * 256 + qrec + wave) * 512 + ksp * 128 + (2 * c + n) * 16, f2u(sel(n, acc.z, acc.x)), f2u(sel(n, acc.w, acc.y)), t0, t1);
      CBAR();
      prog[wave] = bb ? (unsigned)(FKV + 2 * wave) : 4u;
    }
  }
  auto q_reduce = [&](int bb) {
    v4u G;
#pragma unroll 1
    for (;;) {
      CBAR();
      G = ldsc1(rs, (lane & 31) * 16, WS_QR + (bb * 256 + qrec + ksp) * 512);
      if (__all(tagok(G, t0, t1)) || --polls <= 0) break;
    }
    float a0 = u2f(G.x), a1 = u2f(G.z);
    a0 += dpp<ROR8>(a0); a1 += dpp<ROR8>(a1);
    auto s0 = __builtin_amdgcn_permlane16_swap(f2u(a0), f2u(a0), false, false);
    auto s1 = __builtin_amdgcn_permlane16_swap(f2u(a1), f2u(a1), false, false);
    const unsigned pk = pack2(u2f(s0[0]) + u2f(s0[1]), u2f(s1[0]) + u2f(s1[1]));
    const unsigned pn = dppu<XOR1>(pk);
    if (lane < 8 && !(lane & 1)) stpair(rs, WS_QP + (bb * 4 + g) * 4096 + (cg * 16 + ksp * 4 + (lane >> 1)) * 16, pk, pn, t0, t1);
  };
  if (wave == 0) { wait_full(AT_QA); q_reduce(0); }
  const unsigned kofs = (unsigned)(n * 256 + ((c ^ n) << 4));
  const unsigned vofs = (unsigned)((4 * c + q4) * 256 + ((((c & 1) << 2) | q4) << 5) + 8 * p4);
  v4u Kf[2][4], Vf[8];
  auto read_k = [&](LAS unsigned char* sK, int fK) {
    wait_full(fK + 1);
#pragma unroll
    for (int k = 0; k < 4; ++k)
#pragma unroll
      for (int u = 0; u < 2; ++u) Kf[u][k] = *(LAS v4u*)(sK + ((kofs ^ (k << 6)) + u * 4096));
    CBAR();
    prog[wave] = fK + 1;
    CBAR();
    __builtin_amdgcn_sched_barrier(0);
  };
  auto read_v = [&](LAS unsigned char* sK, int fK) {
    wait_full(fK + 2);
#pragma unroll
    for (int e = 0; e < 8; ++e) {
      LAS unsigned char* a = sK + SLOTB + (vofs ^ (e << 5));
      const v2u lo = trread(a), hi = trread(a + 4096);
      Vf[e] = v4u{lo.x, lo.y, hi.x, hi.y};
    }
    CBAR();
    prog[wave] = fK + 2 * NCONS;
    CBAR();
    __builtin_amdgcn_sched_barrier(0);
  };
  read_k(L + (FKV + 2 * wave) * SLOTB, FKV + 2 * wave);
  read_v(L + (FKV + 2 * wave) * SLOTB, FKV + 2 * wave);
  if (wave == 1) { wait_full(AT_QB); q_reduce(1); }
  wait_full(AT_Q);
  {
    v4u G;
#pragma unroll 1
    for (;;) {
      CBAR();
      G = ldsc1(rs, lane * 16, WS_QP + g * 4096 + wave * 1024);
      if (__all(tagok(G, t0, t1)) || --polls <= 0) break;
    }
    *(LAS v2u*)(L + L_Q + (2 * wave + (lane >> 5)) * QSTR + (lane & 31) * 8) = v2u{G.x, G.z};
    arrive(C_Q);
  }
  wait_word(C_Q, ALL4);
  v4u Qf[4];
  auto read_q = [&](int bb) {
#pragma unroll
    for (int k = 0; k < 4; ++k) Qf[k] = *(LAS v4u*)(L + L_Q + (8 * bb + (n & 7)) * QSTR + (4 * k + c) * 16);
  };
  read_q(0);

  float m = -INFINITY, l = 0.f;
  f32x4 O[8];
#pragma unroll
  for (int e = 0; e < 8; ++e) O[e] = f32x4{0.f, 0.f, 0.f, 0.f};
  auto softmax_tile = [&]() {
    f32x4 S0 = {0.f, 0.f, 0.f, 0.f}, S1 = {0.f, 0.f, 0.f, 0.f};
#pragma unroll
    for (int k = 0; k < 4; ++k) {
      S0 = mfma(Kf[0][k], Qf[k], S0);
      S1 = mfma(Kf[1][k], Qf[k], S1);
    }
    const float tm = xrow_max(fmx(fmx(fmx(S0.x, S0.y), fmx(S0.z, S0.w)), fmx(fmx(S1.x, S1.y), fmx(S1.z, S1.w))));
    if (__any(tm > m)) {
      const float mn = fmx(m, tm), al = __builtin_amdgcn_exp2f((m - mn) * C2);
      l *= al;
#pragma unroll
      for (int e = 0; e < 8; ++e) O[e] *= al;
      m = mn;
    }
    const float mb = -m * C2;
    float pp[8];
#pragma unroll
    for (int i = 0; i < 4; ++i) {
      pp[i] = __builtin_amdgcn_exp2f(fmaf(S0[i], C2, mb));
      pp[4 + i] = __builtin_amdgcn_exp2f(fmaf(S1[i], C2, mb));
    }
    l += ((pp[0] + pp[1]) + (pp[2] + pp[3])) + ((pp[4] + pp[5]) + (pp[6] + pp[7]));
    v4u Pf = {pack2(pp[0], pp[1]), pack2(pp[2], pp[3]), pack2(pp[4], pp[5]), pack2(pp[6], pp[7])};
    asm volatile("" : "+v"(Pf), "+v"(l));
    return Pf;
  };
  {
    const v4u Pf = softmax_tile();
#pragma unroll
    for (int e = 0; e < 8; ++e) O[e] = mfma(Vf[e], Pf, O[e]);
  }
  int slot = FKV + 2 * wave;
  auto run = [&](int f0, int f1, int step) {
#pragma unroll 1
    for (int fK = f0; fK < f1; fK += step) {
      LAS unsigned char* sK = L + slot * SLOTB;
      read_k(sK, fK);
      const v4u Pf = softmax_tile();
      read_v(sK, fK);
#pragma unroll
      for (int e = 0; e < 8; ++e) O[e] = mfma(Vf[e], Pf, O[e]);
      slot = slot + step >= NSLOT ? slot + step - NSLOT : slot + step;
    }
  };
  slot = slot + 2 * NCONS >= NSLOT ? slot + 2 * NCONS - NSLOT : slot + 2 * NCONS;
  run(FKV + 2 * (wave + NCONS), FKV + 2 * NCONS * R_BEACON, 2 * NCONS);
  unsigned tb = 0;
  if (wave == 2) {
    tb = (unsigned)__builtin_amdgcn_s_memrealtime();
    if (lane == 0) stpair(rs, WS_BC + b * 16, tb, tb, t0, t1);
  }
  run(FKV + 2 * (wave + NCONS * R_BEACON), FKV + 2 * NCONS * R_STEER, 2 * NCONS);
  if (wave == 2) {
    const v4u G = ldsc1(rs, lane * 16, WS_BC + g * 1024);
    const bool ok = tagok(G, t0, t1);
    float d = ok ? (float)(int)(tb - G.x) : 0.f, cnt = ok ? 1.f : 0.f;
    d += dpp<XOR1>(d); d += dpp<XOR2>(d); d += dpp<ROR4>(d); d += dpp<ROR8>(d); d = xrow_sum(d);
    cnt += dpp<XOR1>(cnt); cnt += dpp<XOR2>(cnt); cnt += dpp<ROR4>(cnt); cnt += dpp<ROR8>(cnt); cnt = xrow_sum(cnt);
    const int lag = (int)(d * __builtin_amdgcn_rcpf(cnt));
    constexpr int fnow = FKV + 2 * NCONS * R_STEER + 8;
    int st = 0;
    if (lag > 20) st = fnow + ((lag * 4) >> 4);
    else if (lag < -20) st = -(fnow + ((-lag * 3) >> 4));
    *(volatile LAS int*)(L + C_B) = st;
  }
  if (wave == 0) {
    v4u G[4];
#pragma unroll 1
    for (;;) {
      CBAR();
      bool ok = true;
#pragma unroll
      for (int j = 0; j < 4; ++j) G[j] = ldsc1(rs, lane * 16 + j * 1024, WS_QP + (4 + g) * 4096);
#pragma unroll
      for (int j = 0; j < 4; ++j) ok &= tagok(G[j], t0, t1);
      if (__all(ok) || --polls <= 0) break;
    }
#pragma unroll
    for (int j = 0; j < 4; ++j) *(LAS v2u*)(L + L_Q + (8 + 2 * j + (lane >> 5)) * QSTR + (lane & 31) * 8) = v2u{G[j].x, G[j].z};
    CBAR();
  }
  run(FKV + 2 * (wave + NCONS * R_STEER), FB, 2 * NCONS);

  constexpr int DEP[4] = {L_DEP, ((FB - 6) % NSLOT) * SLOTB, ((FB - 4) % NSLOT) * SLOTB, ((FB - 2) % NSLOT) * SLOTB};
  static_assert(FB - 6 > NFILL - 1 - NSLOT, "no later fill reuses a deposit slot");
  {
    const float lt = xrow_sum(l);
    if (n < 8) {
      LAS unsigned char* d = L + (wave == 0 ? DEP[0] : wave == 1 ? DEP[1] : wave == 2 ? DEP[2] : DEP[3]) + n * DSTR + c * 16;
#pragma unroll
      for (int e = 0; e < 8; ++e) *(LAS f32x4*)(d + e * 64) = O[e];
      if (c == 0) *(LAS f32x2*)(d + 512) = f32x2{m * C2, lt};
    }
    arrive(C_D);
  }
  if (wave >= 2) {
    const int e = ((lane >> 2) & 6) | (lane & 1), cc = (lane >> 1) & 3;
    float M[2], Ls[2];
    unsigned cs[2];
    wait_word(C_D, ALL4);
#pragma unroll
    for (int j = 0; j < 2; ++j) {
      const int h = 4 * (wave - 2) + 2 * j + (lane >> 5);
      LAS unsigned char* r = L + h * DSTR;
      f32x4 P[4];
      f32x2 ml[4];
#pragma unroll
      for (int w = 0; w < 4; ++w) {
        ml[w] = *(LAS f32x2*)(r + DEP[w] + 512);
        P[w] = *(LAS f32x4*)(r + DEP[w] + e * 64 + cc * 16);
      }
      M[j] = fmx(fmx(ml[0].x, ml[1].x), fmx(ml[2].x, ml[3].x));
      float wt[4];
#pragma unroll
      for (int w = 0; w < 4; ++w) wt[w] = __builtin_amdgcn_exp2f(ml[w].x - M[j]);
      Ls[j] = (wt[0] * ml[0].y + wt[1] * ml[1].y) + (wt[2] * ml[2].y + wt[3] * ml[3].y);
      const f32x4 o = (P[0] * wt[0] + P[1] * wt[1]) + (P[2] * wt[2] + P[3] * wt[3]);
      const unsigned a0 = pack2(o.x, o.y), a1 = pack2(o.z, o.w);
      const v4u d = {a0, a1, dppu<XOR1>(a0), dppu<XOR1>(a1)};
      if (!(lane & 1)) stsc1(rs, WS_PART + (g * 8 + h) * 16384 + ((lane >> 3) & 3) * 4096 + s * 64 + (lane & 7) * 8, d);
      cs[j] = (d.x + d.y) + (d.z + d.w);
      cs[j] += dppu<XOR2>(cs[j]);
      cs[j] += dppu<HMIR>(cs[j]);
    }
    const int j = lane & 1, h = 4 * (wave - 2) + 2 * j + (lane >> 5), rec = WS_ML + ((g * 8 + h) * 64 + s) * 32;
    if (!(lane & 6)) __builtin_amdgcn_raw_buffer_store_b32((j ? cs[1] : cs[0]) + t0, rs, rec + 16 + ((lane >> 3) & 3) * 4, 0, SC1);
    if (!(lane & 30)) {
      stpair(rs, rec, f2u(sel(j, M[1], M[0])), f2u(sel(j, Ls[1], Ls[0])), t0, t1);
      __hip_atomic_fetch_add(counter(0, h), 1u, __ATOMIC_RELAXED, __HIP_MEMORY_SCOPE_AGENT);
    }
  } else if (wave == 0) {
    m = -INFINITY;
    l = 0.f;
#pragma unroll
    for (int e = 0; e < 8; ++e) O[e] = f32x4{0.f, 0.f, 0.f, 0.f};
    read_q(1);
    slot = FB % NSLOT;
    run(FB, FB + 2, 2);
    LAS unsigned char* sK = L + slot * SLOTB;
    read_k(sK, FB + 2);
    const v4u Pf = softmax_tile();
    float lt = xrow_sum(l), mc = m * C2;
    int ob = WS_PART + ((4 + g) * 8 + (n & 7)) * 16384 + (n >> 3) * 4096 + s * 64 + c * 16;
    int oc = WS_ML + ((4 + g) * 512 + s) * 32 + (n & 7) * 2048;
    asm volatile("" : "+v"(lt), "+v"(mc), "+v"(ob), "+v"(oc));
    read_v(sK, FB + 2);
    if (lane < 8) __hip_atomic_fetch_add(counter(1, lane), 1u, __ATOMIC_RELAXED, __HIP_MEMORY_SCOPE_AGENT);
#pragma unroll
    for (int e = 0; e < 8; ++e) O[e] = mfma(Vf[e], Pf, O[e]);
    unsigned cs[2];
#pragma unroll
    for (int i = 0; i < 2; ++i) {
      float v[8];
#pragma unroll
      for (int k = 0; k < 4; ++k) {
        v[k] = sel(n < 8, O[4 * i][k], O[4 * i + 2][k]);
        v[4 + k] = sel(n < 8, O[4 * i + 1][k], O[4 * i + 3][k]);
      }
      const v4u d = {pack2(v[0], v[1]), pack2(v[2], v[3]), pack2(v[4], v[5]), pack2(v[6], v[7])};
      stsc1(rs, ob + i * 8192, d);
      cs[i] = (d.x + d.y) + (d.z + d.w);
    }
    {
      auto x = __builtin_amdgcn_permlane32_swap(cs[0], cs[1], false, false);
      unsigned y = x[0] + x[1];
      auto z = __builtin_amdgcn_permlane16_swap(y, y, false, false);
      y = z[0] + z[1];
      if (!(lane & 16)) __builtin_amdgcn_raw_buffer_store_b32(y + t0, rs, oc + 16 + ((lane >> 5) * 2 + (n >> 3)) * 4, 0, SC1);
    }
    if (lane < 8) stpair(rs, oc, f2u(mc), f2u(lt), t0, t1);
  }

  {
    const int om = WS_ML + ((br * 4 + g) * 8 + hh) * 64 * 32;
    const int od = WS_PART + ((br * 4 + g) * 8 + hh) * 16384 + wave * 4096;
    int vd = lane * 16;
    int oo = WS_OP + ((br * 4 + ksp) * 32 + g * 8 + hh) * 2048 + wave * 512 + lane * 8;
    unsigned aw = lbase + L_W + (lane >> 2) * 4;
    unsigned ao = lbase + L_O + wave * 64 + p4 * 16 + c * 4;
    asm volatile("" : "+v"(vd), "+v"(oo), "+v"(aw), "+v"(ao));
    v4u Wf[16];
    const unsigned wofs = (unsigned)((4 * c + q4) * 512 + 8 * p4), h3 = (unsigned)(q4 | ((c & 1) << 2));
    auto stage = [&](auto ksc) {
      constexpr int ks = decltype(ksc)::value;
      wait_full(FWO + 2 * ks + 2);
      LAS unsigned char* b0 = L + ((FWO + 2 * ks) % NSLOT) * SLOTB + wofs;
#pragma unroll
      for (int k = 0; k < 4; ++k) {
        const unsigned blk = (unsigned)(4 * wave + k), o = (blk >> 3) * 256 + (((blk & 7) ^ h3) << 5);
        const v2u lo = trread(b0 + o), hi = trread(b0 + SLOTB + o);
        Wf[4 * ks + k] = v4u{lo.x, lo.y, hi.x, hi.y};
      }
      __builtin_amdgcn_sched_barrier(0);
    };
    if (wave == 1) {
      wait_full(AT_REC);
      int v, left = HINT_POLLS;
      asm volatile("1:\n\ts_load_dword %0, %2, 0x0 glc\n\ts_waitcnt lgkmcnt(0)\n\ts_cmp_ge_u32 %0, 64\n\ts_cbranch_scc1 2f\n\t"
                   "s_sub_i32 %1, %1, 1\n\ts_cmp_gt_i32 %1, 0\n\ts_sleep 3\n\ts_cbranch_scc1 1b\n2:"
                   : "=&s"(v), "+s"(left) : "s"(counter(br, hh)) : "memory", "scc");
      *(volatile LAS unsigned*)(L + C_M) = 1u;
    } else {
      wait_word(C_M, 1);
    }
    v4u mlg = {0u, 0u, 0u, 0u}, csg = {0u, 0u, 0u, 0u}, D[4];
    if (wave == 1) { mlg = ldsc1(rs, lane * 32, om); csg = ldsc1(rs, lane * 32 + 16, om); }
#pragma unroll
    for (int i = 0; i < 4; ++i) D[i] = ldsc1(rs, vd, od + i * 1024);
    __builtin_amdgcn_sched_barrier(0);
    if (wave == 1) {
      if (!__all(tagok(mlg, t0, t1))) {
        bool ok;
#pragma unroll 1
        do {
          CBAR();
          mlg = ldsc1(rs, lane * 32, om);
          csg = ldsc1(rs, lane * 32 + 16, om);
          asm volatile("" : "+v"(csg));
          ok = __all(tagok(mlg, t0, t1));
        } while (!ok && --polls > 0);
      }
#pragma unroll
      for (int k = 0; k < 4; ++k) *(LAS unsigned*)(L + L_CS + k * 256 + lane * 4) = csg[k];
      const float ms = u2f(mlg.x);
      float M = ms;
      M = fmx(M, dpp<XOR1>(M)); M = fmx(M, dpp<XOR2>(M)); M = fmx(M, dpp<ROR4>(M)); M = fmx(M, dpp<ROR8>(M));
      M = xrow_max(M);
      const float wl = __builtin_amdgcn_exp2f(ms - M);
      float Ls = wl * u2f(mlg.z);
      Ls += dpp<XOR1>(Ls); Ls += dpp<XOR2>(Ls); Ls += dpp<ROR4>(Ls); Ls += dpp<ROR8>(Ls);
      *(LAS float*)(L + L_W + lane * 4) = wl * __builtin_amdgcn_rcpf(xrow_sum(Ls));
      CBAR();
      *(volatile LAS unsigned*)(L + C_W) = 1u;
    }
    stage(IC<0>{});
    stage(IC<1>{});
    wait_word(C_W, 1);
    float wi[4];
    unsigned ck[4];
#pragma unroll
    for (int i = 0; i < 4; ++i) {
      wi[i] = *(LAS float*)(uintptr_t)(aw + i * 64);
      ck[i] = *(LAS unsigned*)(uintptr_t)(aw + (L_CS - L_W) + wave * 256 + i * 64);
    }
    auto intact = [&]() {
      unsigned bad = 0;
#pragma unroll
      for (int i = 0; i < 4; ++i) {
        unsigned x = (D[i].x + D[i].y) + (D[i].z + D[i].w);
        x += dppu<XOR1>(x);
        x += dppu<XOR2>(x);
        bad |= (x + t0) ^ ck[i];
        asm("" : "+v"(bad));
      }
      return (bool)__all(bad == 0);
    };
    if (!intact()) {
      bool ok;
#pragma unroll 1
      do {
        CBAR();
#pragma unroll
        for (int i = 0; i < 4; ++i) {
          ck[i] = __builtin_amdgcn_raw_buffer_load_b32(rs, (lane >> 2) * 32 + 16 + wave * 4, om + i * 512, SC1);
          D[i] = ldsc1(rs, vd, od + i * 1024);
        }
        ok = intact();
      } while (!ok && --polls > 0);
    }
    f32x2 a[4];
#pragma unroll
    for (int k = 0; k < 4; ++k) {
      a[k] = f32x2{u2f(D[0][k] << 16), u2f(D[0][k] & 0xffff0000u)} * wi[0];
#pragma unroll
      for (int i = 1; i < 4; ++i) a[k] += f32x2{u2f(D[i][k] << 16), u2f(D[i][k] & 0xffff0000u)} * wi[i];
    }
    float h4[4], h2[2];
#pragma unroll
    for (int k = 0; k < 4; ++k) {
      auto x = __builtin_amdgcn_permlane32_swap(f2u(a[k >> 1][k & 1]), f2u(a[2 + (k >> 1)][k & 1]), false, false);
      h4[k] = u2f(x[0]) + u2f(x[1]);
    }
#pragma unroll
    for (int k = 0; k < 2; ++k) {
      auto x = __builtin_amdgcn_permlane16_swap(f2u(h4[k]), f2u(h4[2 + k]), false, false);
      h2[k] = u2f(x[0]) + u2f(x[1]);
      h2[k] += dpp<ROR4>(h2[k]);
      h2[k] += dpp<ROR8>(h2[k]);
    }
    *(LAS unsigned*)(uintptr_t)ao = pack2(h2[0], h2[1]);
    arrive(C_O);
    wait_word(C_O, ALL4);
    f32x4 d[4];
#pragma unroll
    for (int k = 0; k < 4; ++k) d[k] = f32x4{0.f, 0.f, 0.f, 0.f};
    v4u Of[4];
#pragma unroll
    for (int ks = 0; ks < 4; ++ks) Of[ks] = *(LAS v4u*)(L + L_O + ks * 64 + c * 16);
    auto kstep = [&](int ks) {
#pragma unroll
      for (int k = 0; k < 4; ++k) d[k] = mfma(Of[ks], Wf[4 * ks + k], d[k]);
    };
    stage(IC<2>{});
    kstep(0);
    kstep(1);
    __builtin_amdgcn_sched_barrier(0);
    stage(IC<3>{});
    kstep(2);
    kstep(3);
    const float r = sel(c & 2, sel(c & 1, d[3].x, d[2].x), sel(c & 1, d[1].x, d[0].x));
    const float rn = dpp<XOR1>(r);
    if (!(lane & 1)) stpair(rs, oo, f2u(r), f2u(rn), t0, t1);
  }
  if (wave != 0) return;

  {
    const int j2 = ((b >> 3) & 1) | ((b & 7) << 1) | (b & 16), set = b >> 5;
    const int of = WS_OP + set * 32 * 2048 + j2 * 64, vf = (lane >> 2) * 2048 + p4 * 16;
    v4u F[2];
#pragma unroll 1
    for (;;) {
      CBAR();
      F[0] = ldsc1(rs, vf, of);
      F[1] = ldsc1(rs, vf, of + 16 * 2048);
      if (__all(tagok(F[0], t0, t1) && tagok(F[1], t0, t1)) || --polls <= 0) break;
    }
    float a0 = u2f(F[0].x) + u2f(F[1].x), a1 = u2f(F[0].z) + u2f(F[1].z);
    a0 += dpp<ROR4>(a0); a1 += dpp<ROR4>(a1);
    a0 += dpp<ROR8>(a0); a1 += dpp<ROR8>(a1);
    a0 = xrow_sum(a0); a1 = xrow_sum(a1);
    if (lane < 4) p.out[set * 128 + j2 * 4 + lane] = pack2(a0, a1);
    if (b == 0 && lane == 0) __hip_atomic_store((GAS unsigned long long*)p.ws, tg, __ATOMIC_RELAXED, __HIP_MEMORY_SCOPE_AGENT);
  }
}
}

extern "C" void kernel_launch(void* const* d_in, const int* in_sizes, int n_in, void* d_out, int out_size, void* d_ws, size_t ws_size,
                              hipStream_t stream) {
  (void)in_sizes; (void)n_in; (void)out_size; (void)ws_size;
  fa::Params p;
  p.xA = (const unsigned char*)d_in[0]; p.kA = (const unsigned char*)d_in[1]; p.vA = (const unsigned char*)d_in[2];
  p.wqA = (const unsigned char*)d_in[3]; p.woA = (const unsigned char*)d_in[4];
  p.xB = (const unsigned char*)d_in[5]; p.kB = (const unsigned char*)d_in[6]; p.vB = (const unsigned char*)d_in[7];
  p.wqB = (const unsigned char*)d_in[8]; p.woB = (const unsigned char*)d_in[9];
  p.out = (unsigned*)d_out;
  p.ws = (unsigned char*)d_ws;
  fa::fused<<<fa::NWG, fa::NTHR, 0, stream>>>(p);
}
```

```cpp
#include <hip/hip_runtime.h>
#include <hip/hip_bf16.h>
#include <stdint.h>

#define GAS __attribute__((address_space(1)))
#define LAS __attribute__((address_space(3)))

namespace fa {
typedef unsigned v4u __attribute__((ext_vector_type(4)));
typedef unsigned v2u __attribute__((ext_vector_type(2)));
typedef float f32x4 __attribute__((ext_vector_type(4)));
typedef float f32x2 __attribute__((ext_vector_type(2)));
typedef short s16x4 __attribute__((ext_vector_type(4)));
typedef __bf16 bf16x8 __attribute__((ext_vector_type(8)));
typedef __bf16 bf16x2 __attribute__((ext_vector_type(2)));

constexpr int SK = 65536, WINB = 4096;
constexpr int NWG = 256, NCONS = 4, NTHR = (NCONS + 1) * 64;
constexpr int NSLOT = 18, SLOTB = 8192;
constexpr int INFL = 2, WIDE = 3;
constexpr int NTA = 32;
constexpr int FKV = 8, FB = FKV + 2 * NTA, FWO = FB + 4, NFILL = FWO + 8;
static_assert(FKV % 4 == 0 && FB % 4 == 0 && NSLOT % 2 == 0, "tiles come in pairs, K and V of a tile in neighbouring slots");
constexpr int AT_QA = 8, AT_QB = 13, AT_Q = 19, AT_REC = NFILL - 6;
constexpr int R_BEACON = 4, R_STEER = 5;
constexpr float C2 = 0.08838834764831845f * 1.4426950408889634f;

constexpr int QSTR = 272, DSTR = 528;
constexpr int L_Q = NSLOT * SLOTB, L_X = L_Q + 2 * 8 * QSTR, L_O = L_X + 1024, L_W = L_O + 256, L_CS = L_W + 256, L_DEP = L_CS + 1024,
              L_CTL = L_DEP + 8 * DSTR, L_END = L_CTL + 48;
static_assert(L_END <= 160 * 1024 && (L_X % 16) == 0 && (L_O % 16) == 0 && (L_DEP % 16) == 0 && (L_CTL % 16) == 0, "lds map");
constexpr int C_FULL = L_CTL + 16, C_M = L_CTL + 20, C_W = L_CTL + 24, C_Q = L_CTL + 28, C_D = L_CTL + 32, C_O = L_CTL + 36, C_B = L_CTL + 40,
              ALL4 = 0x01010101;
constexpr int WS_QR = 4096, WS_QP = WS_QR + 2 * 256 * 512, WS_ML = WS_QP + 2 * 4 * 4096, WS_PART = WS_ML + 2 * 4 * 8 * 64 * 32,
              WS_OP = WS_PART + 2 * 4 * 8 * 64 * 256, WS_BC = WS_OP + 2 * 4 * 32 * 2048, WS_CT = WS_BC + 4 * 1024, WS_END = WS_CT + 64 * 128;
constexpr int SC1 = 16;
constexpr int SPIN_BUDGET = 1 << 22, POLL_BUDGET = 1 << 19;
constexpr int HINT_POLLS = 1 << 14;

struct Params {
  const unsigned char *wqA, *wqB, *xA, *xB, *kA, *vA, *kB, *vB, *woA, *woB;
  unsigned* out;
  unsigned char* ws;
};

template <int V> struct IC { static constexpr int value = V; };
#define CBAR() asm volatile("" ::: "memory")
template <int N> __device__ __forceinline__ void waitvm() { asm volatile("s_waitcnt vmcnt(%0)" ::"i"(N) : "memory"); }
__device__ __forceinline__ void fill8(const unsigned char* ubase, const unsigned (&vo)[8], unsigned dst) {
  asm volatile(
      "s_mov_b32 m0, %9\n\ts_nop 4\n\t"
      "global_load_lds_dwordx4 %0, %8 nt\n\tglobal_load_lds_dwordx4 %1, %8 offset:1024 nt\n\t"
      "global_load_lds_dwordx4 %2, %8 offset:2048 nt\n\tglobal_load_lds_dwordx4 %3, %8 offset:3072 nt\n\t"
      "s_or_b32 m0, %9, 0x1000\n\ts_nop 0\n\t"
      "global_load_lds_dwordx4 %4, %8 nt\n\tglobal_load_lds_dwordx4 %5, %8 offset:1024 nt\n\t"
      "global_load_lds_dwordx4 %6, %8 offset:2048 nt\n\tglobal_load_lds_dwordx4 %7, %8 offset:3072 nt"
      ::"v"(vo[0]), "v"(vo[1]), "v"(vo[2]), "v"(vo[3]), "v"(vo[4]), "v"(vo[5]), "v"(vo[6]), "v"(vo[7]), "s"(ubase), "s"(dst)
      : "memory", "scc");
}
__device__ __forceinline__ f32x4 mfma(v4u a, v4u b, f32x4 c) {
  return __builtin_amdgcn_mfma_f32_16x16x32_bf16(__builtin_bit_cast(bf16x8, a), __builtin_bit_cast(bf16x8, b), c, 0, 0, 0);
}
__device__ __forceinline__ v2u trread(LAS unsigned char* p) {
  return __builtin_bit_cast(v2u, __builtin_amdgcn_ds_read_tr16_b64_v4i16((LAS s16x4*)p));
}
__device__ __forceinline__ unsigned pack2(float a, float b) {
  f32x2 t = {a, b};
  return __builtin_bit_cast(unsigned, __builtin_convertvector(t, bf16x2));
}
__device__ __forceinline__ float u2f(unsigned x) { return __builtin_bit_cast(float, x); }
__device__ __forceinline__ unsigned f2u(float x) { return __builtin_bit_cast(unsigned, x); }
__device__ __forceinline__ float fmx(float a, float b) { return __builtin_elementwise_maximum(a, b); }
template <int CTRL> __device__ __forceinline__ unsigned dppu(unsigned x) { return (unsigned)__builtin_amdgcn_mov_dpp((int)x, CTRL, 0xf, 0xf, true); }
template <int CTRL> __device__ __forceinline__ float dpp(float x) { return u2f(dppu<CTRL>(f2u(x))); }
constexpr int HMIR = 0x141, XOR1 = 0xB1, XOR2 = 0x4E, ROR4 = 0x124, ROR8 = 0x128;
__device__ __forceinline__ float xrow_max(float x) {
  auto s = __builtin_amdgcn_permlane16_swap(f2u(x), f2u(x), false, false);
  x = fmx(u2f(s[0]), u2f(s[1]));
  auto t = __builtin_amdgcn_permlane32_swap(f2u(x), f2u(x), false, false);
  return fmx(u2f(t[0]), u2f(t[1]));
}
__device__ __forceinline__ float xrow_sum(float x) {
  auto s = __builtin_amdgcn_permlane16_swap(f2u(x), f2u(x), false, false);
  x = u2f(s[0]) + u2f(s[1]);
  auto t = __builtin_amdgcn_permlane32_swap(f2u(x), f2u(x), false, false);
  return u2f(t[0]) + u2f(t[1]);
}
__device__ __forceinline__ v4u ldsc1(__amdgpu_buffer_rsrc_t rs, int voff, int soff) { return __builtin_amdgcn_raw_buffer_load_b128(rs, voff, soff, SC1); }
__device__ __forceinline__ void stsc1(__amdgpu_buffer_rsrc_t rs, int off, v4u d) { __builtin_amdgcn_raw_buffer_store_b128(d, rs, off, 0, SC1); }
__device__ __forceinline__ void stpair(__amdgpu_buffer_rsrc_t rs, int off, unsigned a, unsigned b, unsigned t0, unsigned t1) { stsc1(rs, off, v4u{a, t0, b, t1}); }
__device__ __forceinline__ float sel(bool c, float a, float b) {
  asm("" : "+v"(a));
  return c ? a : b;
}
__device__ __forceinline__ bool tagok(v4u g, unsigned t0, unsigned t1) { return g.y == t0 && g.w == t1; }

__device__ __forceinline__ void wait_ge(unsigned a, int need, int& seen, int& budget) {
  unsigned v;
  seen = __builtin_amdgcn_readfirstlane(seen);
  budget = __builtin_amdgcn_readfirstlane(budget);
  need = __builtin_amdgcn_readfirstlane(need);
  asm volatile(
      "s_cmp_ge_i32 %0, %4\n\ts_cbranch_scc1 2f\n"
      "1:\n\tds_read_b32 %2, %3\n\ts_waitcnt lgkmcnt(0)\n\tv_readfirstlane_b32 %0, %2\n\t"
      "s_cmp_ge_i32 %0, %4\n\ts_cbranch_scc1 2f\n\t"
      "s_sub_i32 %1, %1, 1\n\ts_cmp_gt_i32 %1, 0\n\ts_sleep 1\n\ts_cbranch_scc1 1b\n"
      "2:"
      : "+s"(seen), "+s"(budget), "=&v"(v) : "v"(a), "s"(need) : "memory", "scc");
}

__global__ void __launch_bounds__(NTHR, 2) fused(Params p) {
  __shared__ __attribute__((aligned(8192))) unsigned char smem[L_END];
  LAS unsigned char* L = (LAS unsigned char*)smem;
  const unsigned lbase = (unsigned)(uintptr_t)L;
  volatile LAS unsigned* prog = (volatile LAS unsigned*)(L + L_CTL);
  const int tid = threadIdx.x, lane = tid & 63, wave = __builtin_amdgcn_readfirstlane(tid >> 6);
  const int b = blockIdx.x, g = b >> 6, s = b & 63;
  const int cg = (s & 7) | ((s >> 5) << 3), ksp = (s >> 3) & 3, br = s >> 5 ? 0 : 1, hh = s & 7;
  int budget = SPIN_BUDGET;

  if (!(wave < NCONS)) {
    __builtin_amdgcn_s_setprio(3);
    unsigned vk[8], vv[8], vw[8];
#pragma unroll
    for (int i = 0; i < 8; ++i)
      vw[i] = (unsigned)(lane >> 3) * 8192u + ((unsigned)(((lane & 7) ^ (((lane >> 4) & 1) << 1)) << 4) ^ ((i & 1) << 6)) + i * 65536u - (i & 3) * 1024u;
    const size_t qoff = ((size_t)ksp * 256 * 4096 + g * 1024 + cg * 64) * 2;
    int pub = 0;
    auto publish = [&](int cnt) { pub = max(pub, cnt); prog[4] = (unsigned)pub; };
#pragma unroll
    for (int f = 0; f < FKV; ++f) {
      fill8((f < 4 ? p.wqA : p.wqB) + qoff + (size_t)(f & 3) * 64 * 8192, vw, lbase + f * SLOTB);
      if (f == 0) {
        const unsigned char* xs = (lane < 32 ? p.xA : p.xB) + ksp * 512 + (lane & 31) * 16;
        asm volatile("s_mov_b32 m0, %1\n\ts_nop 0\n\tglobal_load_lds_dwordx4 %0, off" ::"v"(xs), "s"(lbase + L_X) : "memory");
        if (lane < 12) prog[lane] = 0u;
      }
      if (f == INFL - 1) {
        asm volatile("s_waitcnt lgkmcnt(0)" ::: "memory");
        __builtin_amdgcn_s_barrier();
      }
      if (f >= WIDE) { waitvm<8 * WIDE>(); publish(f + 1 - WIDE); }
    }
#pragma unroll
    for (int i = 0; i < 8; ++i) {
      vk[i] = (unsigned)(lane >> 4) * 256u + ((unsigned)(((lane & 15) ^ (lane >> 4)) << 4) ^ ((i & 3) << 6)) + (i >> 2) * 8192u;
      vv[i] = (unsigned)(lane >> 4) * 256u + ((unsigned)(((lane & 15) ^ ((lane >> 4) << 1)) << 4) ^ ((i & 1) << 7)) + (i >> 2) * 8192u;
    }
    auto minprog = [&]() {
      const v4u pr = *(volatile LAS v4u*)(L + L_CTL);
      return (int)__builtin_amdgcn_readfirstlane(min(min(pr.x, pr.y), min(pr.z, pr.w)));
    };
    auto park = [&](int f, int need) {
      static_assert(WIDE == 3 && INFL <= WIDE, "drain ladder");
      waitvm<16>(); publish(f - 2); if (minprog() > need) return;
      waitvm<8>(); publish(f - 1); if (minprog() > need) return;
      waitvm<0>(); publish(f);
#pragma unroll 1
      while (minprog() <= need && --budget > 0) __builtin_amdgcn_s_sleep(1);
    };
    int steer = 0;
    auto settle = [&](int f, int issued) {
      if (f < steer) { waitvm<8 * WIDE>(); publish(issued - WIDE); }
      else { waitvm<8 * INFL>(); publish(issued - INFL); if (f < -steer) __builtin_amdgcn_s_sleep(6); }
    };
    const int ca = ((s & 7) >> 1) * 16 + (s >> 3) * 2 + (s & 1), cb = (5 * s + 16 * g) & 63;
    const size_t aoff = ((size_t)g * SK + ca * 1024) * 256, boff = ((size_t)g * SK + (SK - WINB) + cb * 64) * 256;
    const unsigned char *kp = p.kA + aoff, *vp = p.vA + aoff;
    int mp = 0;
    unsigned dst = lbase + FKV * SLOTB;
    auto tiles = [&](int f0, int f1) {
#pragma unroll 1
      for (int f = f0; f < f1; f += 2) {
        if (mp <= f + 1 - NSLOT) park(f, f + 1 - NSLOT);
        fill8(kp, vk, dst);
        settle(f, f + 1);
        fill8(vp, vv, dst + SLOTB);
        const v4u pr = *(volatile LAS v4u*)(L + L_CTL);
        const int adv = (f & 2) ? 12288 : 4096;
        kp += adv; vp += adv;
        dst = dst + 2 * SLOTB == lbase + NSLOT * SLOTB ? lbase : dst + 2 * SLOTB;
        const unsigned st = *(volatile LAS unsigned*)(L + C_B);
        settle(f, f + 2);
        mp = (int)__builtin_amdgcn_readfirstlane(min(min(pr.x, pr.y), min(pr.z, pr.w)));
        steer = (int)__builtin_amdgcn_readfirstlane(st);
      }
    };
    tiles(FKV, FB);
#pragma unroll
    for (int i = 0; i < 8; ++i)
      vw[i] = (unsigned)(2 * i + (lane >> 5)) * 2048u + ((((unsigned)lane & 31u) ^ ((unsigned)((2 * i + (lane >> 5)) & 7) << 1)) << 4) - (i & 3) * 1024u;
    kp = p.kB + boff; vp = p.vB + boff;
    tiles(FB, FWO);
    const unsigned char* wo = (br ? p.woB : p.woA) + ((size_t)(g * 8 + hh) * 128 * 1024 + ksp * 256) * 2;
#pragma unroll 1
    for (int f = FWO; f < NFILL; ++f) {
      if (mp <= f - NSLOT) { park(f, f - NSLOT); mp = f - NSLOT + 1; }
      fill8(wo, vw, dst);
      const v4u pr = *(volatile LAS v4u*)(L + L_CTL);
      wo += 32768;
      dst = dst + SLOTB == lbase + NSLOT * SLOTB ? lbase : dst + SLOTB;
      settle(f, f + 1);
      mp = max(mp, (int)__builtin_amdgcn_readfirstlane(min(min(pr.x, pr.y), min(pr.z, pr.w))));
    }
    waitvm<16>(); publish(NFILL - 2);
    waitvm<8>(); publish(NFILL - 1);
    waitvm<0>(); publish(NFILL);
    return;
  }

  const __amdgpu_buffer_rsrc_t rs = __builtin_amdgcn_make_buffer_rsrc((void*)p.ws, 0, WS_END, 0x00020000);
  unsigned long long E = __hip_atomic_load((GAS unsigned long long*)p.ws, __ATOMIC_RELAXED, __HIP_MEMORY_SCOPE_AGENT);
  const int n = lane & 15, c = lane >> 4, q4 = (lane >> 2) & 3, p4 = lane & 3;
  const unsigned ts = (unsigned)__builtin_amdgcn_s_memrealtime();
  __builtin_amdgcn_s_barrier();
  asm volatile("" : "+v"(E)::"memory");
  const unsigned long long tg = E * 0x9E3779B97F4A7C15ull + 0xD1B54A32D192ED03ull;
  const unsigned t0 = __builtin_amdgcn_readfirstlane((unsigned)tg), t1 = __builtin_amdgcn_readfirstlane((unsigned)(tg >> 32));
  int full = 0, polls = POLL_BUDGET;
  auto wait_full = [&](int need) { wait_ge(lbase + C_FULL, need, full, budget); };
  auto wait_word = [&](int word, int need) { int seen = 0; wait_ge(lbase + word, need, seen, budget); };
  auto arrive = [&](int word) { CBAR(); *(volatile LAS unsigned char*)(L + word + wave) = 1; };
  auto counter = [&](int branch, int head) { return (GAS unsigned*)(p.ws + WS_CT + ((branch * 4 + g) * 8 + head) * 128); };

  if (wave == 2 && s < 16) {
    __hip_atomic_store(counter(s >> 3, s & 7), 0u, __ATOMIC_RELAXED, __HIP_MEMORY_SCOPE_AGENT);
    waitvm<0>();
  }
  const int qrec = (g * 16 + cg) * 4;
  {
    const unsigned wofs = (unsigned)((8 * c + q4) * 128 + ((wave ^ (((q4 >> 1) & 1) | ((c & 1) << 1))) << 5) + 8 * p4);
#pragma unroll 1
    for (int bb = 0; bb < 2; ++bb) {
      f32x4 acc = {0.f, 0.f, 0.f, 0.f};
#pragma unroll 1
      for (int f = 4 * bb; f < 4 * bb + 4; ++f) {
        wait_full(f + 1);
        LAS unsigned char* a = L + f * SLOTB + wofs;
        const v2u l0 = trread(a), h0 = trread(a + 512), l1 = trread(a + 4096), h1 = trread(a + 4096 + 512);
        const v4u x0 = *(LAS v4u*)(L + L_X + f * 128 + c * 16), x1 = *(LAS v4u*)(L + L_X + f * 128 + 64 + c * 16);
        acc = mfma(v4u{l0.x, l0.y, h0.x, h0.y}, x0, acc);
        acc = mfma(v4u{l1.x, l1.y, h1.x, h1.y}, x1, acc);
      }
      if (n < 2) stpair(rs, WS_QR + (bb * 256 + qrec + wave) * 512 + ksp * 128 + (2 * c + n) * 16, f2u(sel(n, acc.z, acc.x)), f2u(sel(n, acc.w, acc.y)), t0, t1);
      CBAR();
      prog[wave] = bb ? (unsigned)(FKV + 2 * wave) : 4u;
    }
  }
  auto q_reduce = [&](int bb) {
    v4u G;
#pragma unroll 1
    for (;;) {
      CBAR();
      G = ldsc1(rs, (lane & 31) * 16, WS_QR + (bb * 256 + qrec + ksp) * 512);
      if (__all(tagok(G, t0, t1)) || --polls <= 0) break;
    }
    float a0 = u2f(G.x), a1 = u2f(G.z);
    a0 += dpp<ROR8>(a0); a1 += dpp<ROR8>(a1);
    auto s0 = __builtin_amdgcn_permlane16_swap(f2u(a0), f2u(a0), false, false);
    auto s1 = __builtin_amdgcn_permlane16_swap(f2u(a1), f2u(a1), false, false);
    const unsigned pk = pack2(u2f(s0[0]) + u2f(s0[1]), u2f(s1[0]) + u2f(s1[1]));
    const unsigned pn = dppu<XOR1>(pk);
    if (lane < 8 && !(lane & 1)) stpair(rs, WS_QP + (bb * 4 + g) * 4096 + (cg * 16 + ksp * 4 + (lane >> 1)) * 16, pk, pn, t0, t1);
  };
  if (wave == 0) { wait_full(AT_QA); q_reduce(0); }
  const unsigned kofs = (unsigned)(n * 256 + ((c ^ n) << 4));
  const unsigned vofs = (unsigned)((4 * c + q4) * 256 + ((((c & 1) << 2) | q4) << 5) + 8 * p4);
  v4u Kf[2][4], Vf[8];
  auto read_k = [&](LAS unsigned char* sK, int fK) {
    wait_full(fK + 1);
#pragma unroll
    for (int k = 0; k < 4; ++k)
#pragma unroll
      for (int u = 0; u < 2; ++u) Kf[u][k] = *(LAS v4u*)(sK + ((kofs ^ (k << 6)) + u * 4096));
    CBAR();
    prog[wave] = fK + 1;
    CBAR();
    __builtin_amdgcn_sched_barrier(0);
  };
  auto read_v = [&](LAS unsigned char* sK, int fK) {
    wait_full(fK + 2);
#pragma unroll
    for (int e = 0; e < 8; ++e) {
      LAS unsigned char* a = sK + SLOTB + (vofs ^ (e << 5));
      const v2u lo = trread(a), hi = trread(a + 4096);
      Vf[e] = v4u{lo.x, lo.y, hi.x, hi.y};
    }
    CBAR();
    prog[wave] = fK + 2 * NCONS;
    CBAR();
    __builtin_amdgcn_sched_barrier(0);
  };
  read_k(L + (FKV + 2 * wave) * SLOTB, FKV + 2 * wave);
  read_v(L + (FKV + 2 * wave) * SLOTB, FKV + 2 * wave);
  if (wave == 1) { wait_full(AT_QB); q_reduce(1); }
  wait_full(AT_Q);
  {
    v4u G;
#pragma unroll 1
    for (;;) {
      CBAR();
      G = ldsc1(rs, lane * 16, WS_QP + g * 4096 + wave * 1024);
      if (__all(tagok(G, t0, t1)) || --polls <= 0) break;
    }
    *(LAS v2u*)(L + L_Q + (2 * wave + (lane >> 5)) * QSTR + (lane & 31) * 8) = v2u{G.x, G.z};
    arrive(C_Q);
  }
  wait_word(C_Q, ALL4);
  v4u Qf[4];
  auto read_q = [&](int bb) {
#pragma unroll
    for (int k = 0; k < 4; ++k) Qf[k] = *(LAS v4u*)(L + L_Q + (8 * bb + (n & 7)) * QSTR + (4 * k + c) * 16);
  };
  read_q(0);

  float m = -INFINITY, l = 0.f;
  f32x4 O[8];
#pragma unroll
  for (int e = 0; e < 8; ++e) O[e] = f32x4{0.f, 0.f, 0.f, 0.f};
  auto softmax_tile = [&]() {
    f32x4 S0 = {0.f, 0.f, 0.f, 0.f}, S1 = {0.f, 0.f, 0.f, 0.f};
#pragma unroll
    for (int k = 0; k < 4; ++k) {
      S0 = mfma(Kf[0][k], Qf[k], S0);
      S1 = mfma(Kf[1][k], Qf[k], S1);
    }
    const float tm = xrow_max(fmx(fmx(fmx(S0.x, S0.y), fmx(S0.z, S0.w)), fmx(fmx(S1.x, S1.y), fmx(S1.z, S1.w))));
    if (__any(tm > m)) {
      const float mn = fmx(m, tm), al = __builtin_amdgcn_exp2f((m - mn) * C2);
      l *= al;
#pragma unroll
      for (int e = 0; e < 8; ++e) O[e] *= al;
      m = mn;
    }
    const float mb = -m * C2;
    float pp[8];
#pragma unroll
    for (int i = 0; i < 4; ++i) {
      pp[i] = __builtin_amdgcn_exp2f(fmaf(S0[i], C2, mb));
      pp[4 + i] = __builtin_amdgcn_exp2f(fmaf(S1[i], C2, mb));
    }
    l += ((pp[0] + pp[1]) + (pp[2] + pp[3])) + ((pp[4] + pp[5]) + (pp[6] + pp[7]));
    v4u Pf = {pack2(pp[0], pp[1]), pack2(pp[2], pp[3]), pack2(pp[4], pp[5]), pack2(pp[6], pp[7])};
    asm volatile("" : "+v"(Pf), "+v"(l));
    return Pf;
  };
  {
    const v4u Pf = softmax_tile();
#pragma unroll
    for (int e = 0; e < 8; ++e) O[e] = mfma(Vf[e], Pf, O[e]);
  }
  int slot = FKV + 2 * wave;
  auto run = [&](int f0, int f1, int step) {
#pragma unroll 1
    for (int fK = f0; fK < f1; fK += step) {
      LAS unsigned char* sK = L + slot * SLOTB;
      read_k(sK, fK);
      const v4u Pf = softmax_tile();
      read_v(sK, fK);
#pragma unroll
      for (int e = 0; e < 8; ++e) O[e] = mfma(Vf[e], Pf, O[e]);
      slot = slot + step >= NSLOT ? slot + step - NSLOT : slot + step;
    }
  };
  slot = slot + 2 * NCONS >= NSLOT ? slot + 2 * NCONS - NSLOT : slot + 2 * NCONS;
  run(FKV + 2 * (wave + NCONS), FKV + 2 * NCONS * R_BEACON, 2 * NCONS);
  unsigned tb = 0;
  if (wave == 2) {
    tb = (unsigned)__builtin_amdgcn_s_memrealtime();
    if (lane == 0) stpair(rs, WS_BC + b * 16, tb, ts, t0, t1);
  }
  run(FKV + 2 * (wave + NCONS * R_BEACON), FKV + 2 * NCONS * R_STEER, 2 * NCONS);
  if (wave == 2) {
    const v4u G = ldsc1(rs, lane * 16, WS_BC + g * 1024);
    const bool ok = tagok(G, t0, t1);
    float d = ok ? (float)(int)(tb - G.x) - 0.375f * (float)(int)(ts - G.z) : 0.f, cnt = ok ? 1.f : 0.f;
    d += dpp<XOR1>(d); d += dpp<XOR2>(d); d += dpp<ROR4>(d); d += dpp<ROR8>(d); d = xrow_sum(d);
    cnt += dpp<XOR1>(cnt); cnt += dpp<XOR2>(cnt); cnt += dpp<ROR4>(cnt); cnt += dpp<ROR8>(cnt); cnt = xrow_sum(cnt);
    const int lag = (int)(d * __builtin_amdgcn_rcpf(cnt));
    constexpr int fnow = FKV + 2 * NCONS * R_STEER + 8;
    int st = 0;
    if (lag > 20) st = fnow + ((lag * 4) >> 4);
    else if (lag < -20) st = -(fnow + ((-lag * 4) >> 4));
    *(volatile LAS int*)(L + C_B) = st;
  }
  if (wave == 0) {
    v4u G[4];
#pragma unroll 1
    for (;;) {
      CBAR();
      bool ok = true;
#pragma unroll
      for (int j = 0; j < 4; ++j) G[j] = ldsc1(rs, lane * 16 + j * 1024, WS_QP + (4 + g) * 4096);
#pragma unroll
      for (int j = 0; j < 4; ++j) ok &= tagok(G[j], t0, t1);
      if (__all(ok) || --polls <= 0) break;
    }
#pragma unroll
    for (int j = 0; j < 4; ++j) *(LAS v2u*)(L + L_Q + (8 + 2 * j + (lane >> 5)) * QSTR + (lane & 31) * 8) = v2u{G[j].x, G[j].z};
    CBAR();
  }
  run(FKV + 2 * (wave + NCONS * R_STEER), FB, 2 * NCONS);

  constexpr int DEP[4] = {L_DEP, ((FB - 6) % NSLOT) * SLOTB, ((FB - 4) % NSLOT) * SLOTB, ((FB - 2) % NSLOT) * SLOTB};
  static_assert(FB - 6 > NFILL - 1 - NSLOT, "no later fill reuses a deposit slot");
  {
    const float lt = xrow_sum(l);
    if (n < 8) {
      LAS unsigned char* d = L + (wave == 0 ? DEP[0] : wave == 1 ? DEP[1] : wave == 2 ? DEP[2] : DEP[3]) + n * DSTR + c * 16;
#pragma unroll
      for (int e = 0; e < 8; ++e) *(LAS f32x4*)(d + e * 64) = O[e];
      if (c == 0) *(LAS f32x2*)(d + 512) = f32x2{m * C2, lt};
    }
    arrive(C_D);
  }
  if (wave >= 2) {
    const int e = ((lane >> 2) & 6) | (lane & 1), cc = (lane >> 1) & 3;
    float M[2], Ls[2];
    unsigned cs[2];
    wait_word(C_D, ALL4);
#pragma unroll
    for (int j = 0; j < 2; ++j) {
      const int h = 4 * (wave - 2) + 2 * j + (lane >> 5);
      LAS unsigned char* r = L + h * DSTR;
      f32x4 P[4];
      f32x2 ml[4];
#pragma unroll
      for (int w = 0; w < 4; ++w) {
        ml[w] = *(LAS f32x2*)(r + DEP[w] + 512);
        P[w] = *(LAS f32x4*)(r + DEP[w] + e * 64 + cc * 16);
      }
      M[j] = fmx(fmx(ml[0].x, ml[1].x), fmx(ml[2].x, ml[3].x));
      float wt[4];
#pragma unroll
      for (int w = 0; w < 4; ++w) wt[w] = __builtin_amdgcn_exp2f(ml[w].x - M[j]);
      Ls[j] = (wt[0] * ml[0].y + wt[1] * ml[1].y) + (wt[2] * ml[2].y + wt[3] * ml[3].y);
      const f32x4 o = (P[0] * wt[0] + P[1] * wt[1]) + (P[2] * wt[2] + P[3] * wt[3]);
      const unsigned a0 = pack2(o.x, o.y), a1 = pack2(o.z, o.w);
      const v4u d = {a0, a1, dppu<XOR1>(a0), dppu<XOR1>(a1)};
      if (!(lane & 1)) stsc1(rs, WS_PART + (g * 8 + h) * 16384 + ((lane >> 3) & 3) * 4096 + s * 64 + (lane & 7) * 8, d);
      cs[j] = (d.x + d.y) + (d.z + d.w);
      cs[j] += dppu<XOR2>(cs[j]);
      cs[j] += dppu<HMIR>(cs[j]);
    }
    const int j = lane & 1, h = 4 * (wave - 2) + 2 * j + (lane >> 5), rec = WS_ML + ((g * 8 + h) * 64 + s) * 32;
    if (!(lane & 6)) __builtin_amdgcn_raw_buffer_store_b32((j ? cs[1] : cs[0]) + t0, rs, rec + 16 + ((lane >> 3) & 3) * 4, 0, SC1);
    if (!(lane & 30)) {
      stpair(rs, rec, f2u(sel(j, M[1], M[0])), f2u(sel(j, Ls[1], Ls[0])), t0, t1);
      __hip_atomic_fetch_add(counter(0, h), 1u, __ATOMIC_RELAXED, __HIP_MEMORY_SCOPE_AGENT);
    }
  } else if (wave == 0) {
    m = -INFINITY;
    l = 0.f;
#pragma unroll
    for (int e = 0; e < 8; ++e) O[e] = f32x4{0.f, 0.f, 0.f, 0.f};
    read_q(1);
    slot = FB % NSLOT;
    run(FB, FB + 2, 2);
    LAS unsigned char* sK = L + slot * SLOTB;
    read_k(sK, FB + 2);
    const v4u Pf = softmax_tile();
    float lt = xrow_sum(l), mc = m * C2;
    int ob = WS_PART + ((4 + g) * 8 + (n & 7)) * 16384 + (n >> 3) * 4096 + s * 64 + c * 16;
    int oc = WS_ML + ((4 + g) * 512 + s) * 32 + (n & 7) * 2048;
    asm volatile("" : "+v"(lt), "+v"(mc), "+v"(ob), "+v"(oc));
    read_v(sK, FB + 2);
    if (lane < 8) __hip_atomic_fetch_add(counter(1, lane), 1u, __ATOMIC_RELAXED, __HIP_MEMORY_SCOPE_AGENT);
#pragma unroll
    for (int e = 0; e < 8; ++e) O[e] = mfma(Vf[e], Pf, O[e]);
    unsigned cs[2];
#pragma unroll
    for (int i = 0; i < 2; ++i) {
      float v[8];
#pragma unroll
      for (int k = 0; k < 4; ++k) {
        v[k] = sel(n < 8, O[4 * i][k], O[4 * i + 2][k]);
        v[4 + k] = sel(n < 8, O[4 * i + 1][k], O[4 * i + 3][k]);
      }
      const v4u d = {pack2(v[0], v[1]), pack2(v[2], v[3]), pack2(v[4], v[5]), pack2(v[6], v[7])};
      stsc1(rs, ob + i * 8192, d);
      cs[i] = (d.x + d.y) + (d.z + d.w);
    }
    {
      auto x = __builtin_amdgcn_permlane32_swap(cs[0], cs[1], false, false);
      unsigned y = x[0] + x[1];
      auto z = __builtin_amdgcn_permlane16_swap(y, y, false, false);
      y = z[0] + z[1];
      if (!(lane & 16)) __builtin_amdgcn_raw_buffer_store_b32(y + t0, rs, oc + 16 + ((lane >> 5) * 2 + (n >> 3)) * 4, 0, SC1);
    }
    if (lane < 8) stpair(rs, oc, f2u(mc), f2u(lt), t0, t1);
  }

  {
    const int om = WS_ML + ((br * 4 + g) * 8 + hh) * 64 * 32;
    const int od = WS_PART + ((br * 4 + g) * 8 + hh) * 16384 + wave * 4096;
    int vd = lane * 16;
    int oo = WS_OP + ((br * 4 + ksp) * 32 + g * 8 + hh) * 2048 + wave * 512 + lane * 8;
    unsigned aw = lbase + L_W + (lane >> 2) * 4;
    unsigned ao = lbase + L_O + wave * 64 + p4 * 16 + c * 4;
    asm volatile("" : "+v"(vd), "+v"(oo), "+v"(aw), "+v"(ao));
    v4u Wf[16];
    const unsigned wofs = (unsigned)((4 * c + q4) * 512 + 8 * p4), h3 = (unsigned)(q4 | ((c & 1) << 2));
    auto stage = [&](auto ksc) {
      constexpr int ks = decltype(ksc)::value;
      wait_full(FWO + 2 * ks + 2);
      LAS unsigned char* b0 = L + ((FWO + 2 * ks) % NSLOT) * SLOTB + wofs;
#pragma unroll
      for (int k = 0; k < 4; ++k) {
        const unsigned blk = (unsigned)(4 * wave + k), o = (blk >> 3) * 256 + (((blk & 7) ^ h3) << 5);
        const v2u lo = trread(b0 + o), hi = trread(b0 + SLOTB + o);
        Wf[4 * ks + k] = v4u{lo.x, lo.y, hi.x, hi.y};
      }
      __builtin_amdgcn_sched_barrier(0);
    };
    if (wave == 1) {
      wait_full(AT_REC);
      int v, left = HINT_POLLS;
      asm volatile("1:\n\ts_load_dword %0, %2, 0x0 glc\n\ts_waitcnt lgkmcnt(0)\n\ts_cmp_ge_u32 %0, 64\n\ts_cbranch_scc1 2f\n\t"
                   "s_sub_i32 %1, %1, 1\n\ts_cmp_gt_i32 %1, 0\n\ts_sleep 3\n\ts_cbranch_scc1 1b\n2:"
                   : "=&s"(v), "+s"(left) : "s"(counter(br, hh)) : "memory", "scc");
      *(volatile LAS unsigned*)(L + C_M) = 1u;
    } else {
      wait_word(C_M, 1);
    }
    v4u mlg = {0u, 0u, 0u, 0u}, csg = {0u, 0u, 0u, 0u}, D[4];
    if (wave == 1) { mlg = ldsc1(rs, lane * 32, om); csg = ldsc1(rs, lane * 32 + 16, om); }
#pragma unroll
    for (int i = 0; i < 4; ++i) D[i] = ldsc1(rs, vd, od + i * 1024);
    __builtin_amdgcn_sched_barrier(0);
    if (wave == 1) {
      if (!__all(tagok(mlg, t0, t1))) {
        bool ok;
#pragma unroll 1
        do {
          CBAR();
          mlg = ldsc1(rs, lane * 32, om);
          csg = ldsc1(rs, lane * 32 + 16, om);
          asm volatile("" : "+v"(csg));
          ok = __all(tagok(mlg, t0, t1));
        } while (!ok && --polls > 0);
      }
#pragma unroll
      for (int k = 0; k < 4; ++k) *(LAS unsigned*)(L + L_CS + k * 256 + lane * 4) = csg[k];
      const float ms = u2f(mlg.x);
      float M = ms;
      M = fmx(M, dpp<XOR1>(M)); M = fmx(M, dpp<XOR2>(M)); M = fmx(M, dpp<ROR4>(M)); M = fmx(M, dpp<ROR8>(M));
      M = xrow_max(M);
      const float wl = __builtin_amdgcn_exp2f(ms - M);
      float Ls = wl * u2f(mlg.z);
      Ls += dpp<XOR1>(Ls); Ls += dpp<XOR2>(Ls); Ls += dpp<ROR4>(Ls); Ls += dpp<ROR8>(Ls);
      *(LAS float*)(L + L_W + lane * 4) = wl * __builtin_amdgcn_rcpf(xrow_sum(Ls));
      CBAR();
      *(volatile LAS unsigned*)(L + C_W) = 1u;
    }
    stage(IC<0>{});
    stage(IC<1>{});
    wait_word(C_W, 1);
    float wi[4];
    unsigned ck[4];
#pragma unroll
    for (int i = 0; i < 4; ++i) {
      wi[i] = *(LAS float*)(uintptr_t)(aw + i * 64);
      ck[i] = *(LAS unsigned*)(uintptr_t)(aw + (L_CS - L_W) + wave * 256 + i * 64);
    }
    auto intact = [&]() {
      unsigned bad = 0;
#pragma unroll
      for (int i = 0; i < 4; ++i) {
        unsigned x = (D[i].x + D[i].y) + (D[i].z + D[i].w);
        x += dppu<XOR1>(x);
        x += dppu<XOR2>(x);
        bad |= (x + t0) ^ ck[i];
        asm("" : "+v"(bad));
      }
      return (bool)__all(bad == 0);
    };
    if (!intact()) {
      bool ok;
#pragma unroll 1
      do {
        CBAR();
#pragma unroll
        for (int i = 0; i < 4; ++i) {
          ck[i] = __builtin_amdgcn_raw_buffer_load_b32(rs, (lane >> 2) * 32 + 16 + wave * 4, om + i * 512, SC1);
          D[i] = ldsc1(rs, vd, od + i * 1024);
        }
        ok = intact();
      } while (!ok && --polls > 0);
    }
    f32x2 a[4];
#pragma unroll
    for (int k = 0; k < 4; ++k) {
      a[k] = f32x2{u2f(D[0][k] << 16), u2f(D[0][k] & 0xffff0000u)} * wi[0];
#pragma unroll
      for (int i = 1; i < 4; ++i) a[k] += f32x2{u2f(D[i][k] << 16), u2f(D[i][k] & 0xffff0000u)} * wi[i];
    }
    float h4[4], h2[2];
#pragma unroll
    for (int k = 0; k < 4; ++k) {
      auto x = __builtin_amdgcn_permlane32_swap(f2u(a[k >> 1][k & 1]), f2u(a[2 + (k >> 1)][k & 1]), false, false);
      h4[k] = u2f(x[0]) + u2f(x[1]);
    }
#pragma unroll
    for (int k = 0; k < 2; ++k) {
      auto x = __builtin_amdgcn_permlane16_swap(f2u(h4[k]), f2u(h4[2 + k]), false, false);
      h2[k] = u2f(x[0]) + u2f(x[1]);
      h2[k] += dpp<ROR4>(h2[k]);
      h2[k] += dpp<ROR8>(h2[k]);
    }
    *(LAS unsigned*)(uintptr_t)ao = pack2(h2[0], h2[1]);
    arrive(C_O);
    wait_word(C_O, ALL4);
    f32x4 d[4];
#pragma unroll
    for (int k = 0; k < 4; ++k) d[k] = f32x4{0.f, 0.f, 0.f, 0.f};
    v4u Of[4];
#pragma unroll
    for (int ks = 0; ks < 4; ++ks) Of[ks] = *(LAS v4u*)(L + L_O + ks * 64 + c * 16);
    auto kstep = [&](int ks) {
#pragma unroll
      for (int k = 0; k < 4; ++k) d[k] = mfma(Of[ks], Wf[4 * ks + k], d[k]);
    };
    stage(IC<2>{});
    kstep(0);
    kstep(1);
    __builtin_amdgcn_sched_barrier(0);
    stage(IC<3>{});
    kstep(2);
    kstep(3);
    const float r = sel(c & 2, sel(c & 1, d[3].x, d[2].x), sel(c & 1, d[1].x, d[0].x));
    const float rn = dpp<XOR1>(r);
    if (!(lane & 1)) stpair(rs, oo, f2u(r), f2u(rn), t0, t1);
  }
  if (wave != 0) return;

  {
    const int j2 = ((b >> 3) & 1) | ((b & 7) << 1) | (b & 16), set = b >> 5;
    const int of = WS_OP + set * 32 * 2048 + j2 * 64, vf = (lane >> 2) * 2048 + p4 * 16;
    v4u F[2];
#pragma unroll 1
    for (;;) {
      CBAR();
      F[0] = ldsc1(rs, vf, of);
      F[1] = ldsc1(rs, vf, of + 16 * 2048);
      if (__all(tagok(F[0], t0, t1) && tagok(F[1], t0, t1)) || --polls <= 0) break;
    }
    float a0 = u2f(F[0].x) + u2f(F[1].x), a1 = u2f(F[0].z) + u2f(F[1].z);
    a0 += dpp<ROR4>(a0); a1 += dpp<ROR4>(a1);
    a0 += dpp<ROR8>(a0); a1 += dpp<ROR8>(a1);
    a0 = xrow_sum(a0); a1 = xrow_sum(a1);
    if (lane < 4) p.out[set * 128 + j2 * 4 + lane] = pack2(a0, a1);
    if (b == 0 && lane == 0) __hip_atomic_store((GAS unsigned long long*)p.ws, tg, __ATOMIC_RELAXED, __HIP_MEMORY_SCOPE_AGENT);
  }
}
}

extern "C" void kernel_launch(void* const* d_in, const int* in_sizes, int n_in, void* d_out, int out_size, void* d_ws, size_t ws_size,
                              hipStream_t stream) {
  (void)in_sizes; (void)n_in; (void)out_size; (void)ws_size;
  fa::Params p;
  p.xA = (const unsigned char*)d_in[0]; p.kA = (const unsigned char*)d_in[1]; p.vA = (const unsigned char*)d_in[2];
  p.wqA = (const unsigned char*)d_in[3]; p.woA = (const unsigned char*)d_in[4];
  p.xB = (const unsigned char*)d_in[5]; p.kB = (const unsigned char*)d_in[6]; p.vB = (const unsigned char*)d_in[7];
  p.wqB = (const unsigned char*)d_in[8]; p.woB = (const unsigned char*)d_in[9];
  p.out = (unsigned*)d_out;
  p.ws = (unsigned char*)d_ws;
  fa::fused<<<fa::NWG, fa::NTHR, 0, stream>>>(p);
}
```

```cpp
#include <hip/hip_runtime.h>
#include <hip/hip_bf16.h>
#include <stdint.h>

#define GAS __attribute__((address_space(1)))
#define LAS __attribute__((address_space(3)))

namespace fa {
typedef unsigned v4u __attribute__((ext_vector_type(4)));
typedef unsigned v2u __attribute__((ext_vector_type(2)));
typedef float f32x4 __attribute__((ext_vector_type(4)));
typedef float f32x2 __attribute__((ext_vector_type(2)));
typedef short s16x4 __attribute__((ext_vector_type(4)));
typedef __bf16 bf16x8 __attribute__((ext_vector_type(8)));
typedef __bf16 bf16x2 __attribute__((ext_vector_type(2)));

constexpr int SK = 65536, WINB = 4096;
constexpr int NWG = 256, NCONS = 4, NTHR = (NCONS + 1) * 64;
constexpr int NSLOT = 18, SLOTB = 8192;
constexpr int INFL = 2, WIDE = 3;
constexpr int NTA = 32;
constexpr int FKV = 8, FB = FKV + 2 * NTA, FWO = FB + 4, NFILL = FWO + 8;
static_assert(FKV % 4 == 0 && FB % 4 == 0 && NSLOT % 2 == 0, "tiles come in pairs, K and V of a tile in neighbouring slots");
constexpr int AT_QA = 8, AT_QB = 13, AT_Q = 19, AT_REC = NFILL - 6;
constexpr int R_BEACON = 4, R_STEER = 5;
constexpr float C2 = 0.08838834764831845f * 1.4426950408889634f;

constexpr int QSTR = 272, DSTR = 528;
constexpr int L_Q = NSLOT * SLOTB, L_X = L_Q + 2 * 8 * QSTR, L_O = L_X + 1024, L_W = L_O + 256, L_CS = L_W + 272, L_DEP = L_CS + 1024,
              L_CTL = L_DEP + 8 * DSTR, L_END = L_CTL + 48;
static_assert(L_END <= 160 * 1024 && (L_X % 16) == 0 && (L_O % 16) == 0 && (L_DEP % 16) == 0 && (L_CTL % 16) == 0, "lds map");
constexpr int C_FULL = L_CTL + 16, C_M = L_CTL + 20, C_W = L_CTL + 24, C_Q = L_CTL + 28, C_D = L_CTL + 32, C_O = L_CTL + 36, C_B = L_CTL + 40,
              ALL4 = 0x01010101;
constexpr int WS_QR = 4096, WS_QP = WS_QR + 2 * 256 * 512, WS_ML = WS_QP + 2 * 4 * 4096, WS_PART = WS_ML + 2 * 4 * 8 * 64 * 32,
              WS_OP = WS_PART + 2 * 4 * 8 * 64 * 256, WS_BC = WS_OP + 2 * 4 * 32 * 2048, WS_CT = WS_BC + 4 * 1024, WS_END = WS_CT + 64 * 128;
constexpr int SC1 = 16;
constexpr int SPIN_BUDGET = 1 << 22, POLL_BUDGET = 1 << 19;
constexpr int HINT_POLLS = 1 << 14;

struct Params {
  const unsigned char *wqA, *wqB, *xA, *xB, *kA, *vA, *kB, *vB, *woA, *woB;
  unsigned* out;
  unsigned char* ws;
};

template <int V> struct IC { static constexpr int value = V; };
#define CBAR() asm volatile("" ::: "memory")
template <int N> __device__ __forceinline__ void waitvm() { asm volatile("s_waitcnt vmcnt(%0)" ::"i"(N) : "memory"); }
__device__ __forceinline__ void fill8(const unsigned char* ubase, const unsigned (&vo)[8], unsigned dst) {
  asm volatile(
      "s_mov_b32 m0, %9\n\ts_nop 4\n\t"
      "global_load_lds_dwordx4 %0, %8 nt\n\tglobal_load_lds_dwordx4 %1, %8 offset:1024 nt\n\t"
      "global_load_lds_dwordx4 %2, %8 offset:2048 nt\n\tglobal_load_lds_dwordx4 %3, %8 offset:3072 nt\n\t"
      "s_or_b32 m0, %9, 0x1000\n\ts_nop 0\n\t"
      "global_load_lds_dwordx4 %4, %8 nt\n\tglobal_load_lds_dwordx4 %5, %8 offset:1024 nt\n\t"
      "global_load_lds_dwordx4 %6, %8 offset:2048 nt\n\tglobal_load_lds_dwordx4 %7, %8 offset:3072 nt"
      ::"v"(vo[0]), "v"(vo[1]), "v"(vo[2]), "v"(vo[3]), "v"(vo[4]), "v"(vo[5]), "v"(vo[6]), "v"(vo[7]), "s"(ubase), "s"(dst)
      : "memory", "scc");
}
__device__ __forceinline__ f32x4 mfma(v4u a, v4u b, f32x4 c) {
  return __builtin_amdgcn_mfma_f32_16x16x32_bf16(__builtin_bit_cast(bf16x8, a), __builtin_bit_cast(bf16x8, b), c, 0, 0, 0);
}
__device__ __forceinline__ v2u trread(LAS unsigned char* p) {
  return __builtin_bit_cast(v2u, __builtin_amdgcn_ds_read_tr16_b64_v4i16((LAS s16x4*)p));
}
__device__ __forceinline__ unsigned pack2(float a, float b) {
  f32x2 t = {a, b};
  return __builtin_bit_cast(unsigned, __builtin_convertvector(t, bf16x2));
}
__device__ __forceinline__ float u2f(unsigned x) { return __builtin_bit_cast(float, x); }
__device__ __forceinline__ unsigned f2u(float x) { return __builtin_bit_cast(unsigned, x); }
__device__ __forceinline__ float fmx(float a, float b) { return __builtin_elementwise_maximum(a, b); }
template <int CTRL> __device__ __forceinline__ unsigned dppu(unsigned x) { return (unsigned)__builtin_amdgcn_mov_dpp((int)x, CTRL, 0xf, 0xf, true); }
template <int CTRL> __device__ __forceinline__ float dpp(float x) { return u2f(dppu<CTRL>(f2u(x))); }
constexpr int HMIR = 0x141, XOR1 = 0xB1, XOR2 = 0x4E, ROR4 = 0x124, ROR8 = 0x128;
__device__ __forceinline__ float xrow_max(float x) {
  auto s = __builtin_amdgcn_permlane16_swap(f2u(x), f2u(x), false, false);
  x = fmx(u2f(s[0]), u2f(s[1]));
  auto t = __builtin_amdgcn_permlane32_swap(f2u(x), f2u(x), false, false);
  return fmx(u2f(t[0]), u2f(t[1]));
}
__device__ __forceinline__ float xrow_sum(float x) {
  auto s = __builtin_amdgcn_permlane16_swap(f2u(x), f2u(x), false, false);
  x = u2f(s[0]) + u2f(s[1]);
  auto t = __builtin_amdgcn_permlane32_swap(f2u(x), f2u(x), false, false);
  return u2f(t[0]) + u2f(t[1]);
}
__device__ __forceinline__ v4u ldsc1(__amdgpu_buffer_rsrc_t rs, int voff, int soff) { return __builtin_amdgcn_raw_buffer_load_b128(rs, voff, soff, SC1); }
__device__ __forceinline__ void stsc1(__amdgpu_buffer_rsrc_t rs, int off, v4u d) { __builtin_amdgcn_raw_buffer_store_b128(d, rs, off, 0, SC1); }
__device__ __forceinline__ void stpair(__amdgpu_buffer_rsrc_t rs, int off, unsigned a, unsigned b, unsigned t0, unsigned t1) { stsc1(rs, off, v4u{a, t0, b, t1}); }
__device__ __forceinline__ float sel(bool c, float a, float b) {
  asm("" : "+v"(a));
  return c ? a : b;
}
__device__ __forceinline__ bool tagok(v4u g, unsigned t0, unsigned t1) { return g.y == t0 && g.w == t1; }

__device__ __forceinline__ void wait_ge(unsigned a, int need, int& seen, int& budget) {
  unsigned v;
  seen = __builtin_amdgcn_readfirstlane(seen);
  budget = __builtin_amdgcn_readfirstlane(budget);
  need = __builtin_amdgcn_readfirstlane(need);
  asm volatile(
      "s_cmp_ge_i32 %0, %4\n\ts_cbranch_scc1 2f\n"
      "1:\n\tds_read_b32 %2, %3\n\ts_waitcnt lgkmcnt(0)\n\tv_readfirstlane_b32 %0, %2\n\t"
      "s_cmp_ge_i32 %0, %4\n\ts_cbranch_scc1 2f\n\t"
      "s_sub_i32 %1, %1, 1\n\ts_cmp_gt_i32 %1, 0\n\ts_cbranch_scc1 1b\n"
      "2:"
      : "+s"(seen), "+s"(budget), "=&v"(v) : "v"(a), "s"(need) : "memory", "scc");
}

__global__ void __launch_bounds__(NTHR, 2) fused(Params p) {
  __shared__ __attribute__((aligned(8192))) unsigned char smem[L_END];
  LAS unsigned char* L = (LAS unsigned char*)smem;
  const unsigned lbase = (unsigned)(uintptr_t)L;
  volatile LAS unsigned* prog = (volatile LAS unsigned*)(L + L_CTL);
  const int tid = threadIdx.x, lane = tid & 63, wave = __builtin_amdgcn_readfirstlane(tid >> 6);
  const int b = blockIdx.x, g = b >> 6, s = b & 63;
  const int cg = (s & 7) | ((s >> 5) << 3), ksp = (s >> 3) & 3, br = s >> 5 ? 0 : 1, hh = s & 7;
  int budget = SPIN_BUDGET;

  if (!(wave < NCONS)) {
    __builtin_amdgcn_s_setprio(3);
    unsigned vk[8], vv[8], vw[8];
#pragma unroll
    for (int i = 0; i < 8; ++i)
      vw[i] = (unsigned)(lane >> 3) * 8192u + ((unsigned)(((lane & 7) ^ (((lane >> 4) & 1) << 1)) << 4) ^ ((i & 1) << 6)) + i * 65536u - (i & 3) * 1024u;
    const size_t qoff = ((size_t)ksp * 256 * 4096 + g * 1024 + cg * 64) * 2;
    int pub = 0;
    auto publish = [&](int cnt) { pub = max(pub, cnt); prog[4] = (unsigned)pub; };
#pragma unroll
    for (int f = 0; f < FKV; ++f) {
      fill8((f < 4 ? p.wqA : p.wqB) + qoff + (size_t)(f & 3) * 64 * 8192, vw, lbase + f * SLOTB);
      if (f == 0) {
        const unsigned char* xs = (lane < 32 ? p.xA : p.xB) + ksp * 512 + (lane & 31) * 16;
        asm volatile("s_mov_b32 m0, %1\n\ts_nop 0\n\tglobal_load_lds_dwordx4 %0, off" ::"v"(xs), "s"(lbase + L_X) : "memory");
        if (lane < 12) prog[lane] = 0u;
      }
      if (f == INFL - 1) {
        asm volatile("s_waitcnt lgkmcnt(0)" ::: "memory");
        __builtin_amdgcn_s_barrier();
      }
      if (f >= WIDE) { waitvm<8 * WIDE>(); publish(f + 1 - WIDE); }
    }
#pragma unroll
    for (int i = 0; i < 8; ++i) {
      vk[i] = (unsigned)(lane >> 4) * 256u + ((unsigned)(((lane & 15) ^ (lane >> 4)) << 4) ^ ((i & 3) << 6)) + (i >> 2) * 8192u;
      vv[i] = (unsigned)(lane >> 4) * 256u + ((unsigned)(((lane & 15) ^ ((lane >> 4) << 1)) << 4) ^ ((i & 1) << 7)) + (i >> 2) * 8192u;
    }
    auto minprog = [&]() {
      const v4u pr = *(volatile LAS v4u*)(L + L_CTL);
      return (int)__builtin_amdgcn_readfirstlane(min(min(pr.x, pr.y), min(pr.z, pr.w)));
    };
    auto park = [&](int f, int need) {
      static_assert(WIDE == 3 && INFL <= WIDE, "drain ladder");
      waitvm<16>(); publish(f - 2); if (minprog() > need) return;
      waitvm<8>(); publish(f - 1); if (minprog() > need) return;
      waitvm<0>(); publish(f);
#pragma unroll 1
      while (minprog() <= need && --budget > 0) __builtin_amdgcn_s_sleep(1);
    };
    int steer = 0;
    auto settle = [&](int f, int issued) {
      if (f < steer) { waitvm<8 * WIDE>(); publish(issued - WIDE); }
      else { waitvm<8 * INFL>(); publish(issued - INFL); if (f < -steer) __builtin_amdgcn_s_sleep(6); }
    };
    const int ca = ((s & 7) >> 1) * 16 + (s >> 3) * 2 + (s & 1), cb = (5 * s + 16 * g) & 63;
    const size_t aoff = ((size_t)g * SK + ca * 1024) * 256, boff = ((size_t)g * SK + (SK - WINB) + cb * 64) * 256;
    const unsigned char *kp = p.kA + aoff, *vp = p.vA + aoff;
    int mp = 0;
    unsigned dst = lbase + FKV * SLOTB;
    auto tiles = [&](int f0, int f1) {
#pragma unroll 1
      for (int f = f0; f < f1; f += 2) {
        if (mp <= f + 1 - NSLOT) park(f, f + 1 - NSLOT);
        fill8(kp, vk, dst);
        settle(f, f + 1);
        fill8(vp, vv, dst + SLOTB);
        const v4u pr = *(volatile LAS v4u*)(L + L_CTL);
        const int adv = (f & 2) ? 12288 : 4096;
        kp += adv; vp += adv;
        dst = dst + 2 * SLOTB == lbase + NSLOT * SLOTB ? lbase : dst + 2 * SLOTB;
        const unsigned st = *(volatile LAS unsigned*)(L + C_B);
        settle(f, f + 2);
        mp = (int)__builtin_amdgcn_readfirstlane(min(min(pr.x, pr.y), min(pr.z, pr.w)));
        steer = (int)__builtin_amdgcn_readfirstlane(st);
      }
    };
    tiles(FKV, FB);
#pragma unroll
    for (int i = 0; i < 8; ++i)
      vw[i] = (unsigned)(2 * i + (lane >> 5)) * 2048u + ((((unsigned)lane & 31u) ^ ((unsigned)((2 * i + (lane >> 5)) & 7) << 1)) << 4) - (i & 3) * 1024u;
    kp = p.kB + boff; vp = p.vB + boff;
    tiles(FB, FWO);
    const unsigned char* wo = (br ? p.woB : p.woA) + ((size_t)(g * 8 + hh) * 128 * 1024 + ksp * 256) * 2;
#pragma unroll 1
    for (int f = FWO; f < NFILL; ++f) {
      if (mp <= f - NSLOT) { park(f, f - NSLOT); mp = f - NSLOT + 1; }
      fill8(wo, vw, dst);
      const v4u pr = *(volatile LAS v4u*)(L + L_CTL);
      wo += 32768;
      dst = dst + SLOTB == lbase + NSLOT * SLOTB ? lbase : dst + SLOTB;
      settle(f, f + 1);
      mp = max(mp, (int)__builtin_amdgcn_readfirstlane(min(min(pr.x, pr.y), min(pr.z, pr.w))));
    }
    waitvm<16>(); publish(NFILL - 2);
    waitvm<8>(); publish(NFILL - 1);
    waitvm<0>(); publish(NFILL);
    return;
  }

  const __amdgpu_buffer_rsrc_t rs = __builtin_amdgcn_make_buffer_rsrc((void*)p.ws, 0, WS_END, 0x00020000);
  unsigned long long E = __hip_atomic_load((GAS unsigned long long*)p.ws, __ATOMIC_RELAXED, __HIP_MEMORY_SCOPE_AGENT);
  const int n = lane & 15, c = lane >> 4, q4 = (lane >> 2) & 3, p4 = lane & 3;
  const unsigned ts = (unsigned)__builtin_amdgcn_s_memrealtime();
  __builtin_amdgcn_s_barrier();
  asm volatile("" : "+v"(E)::"memory");
  const unsigned long long tg = E * 0x9E3779B97F4A7C15ull + 0xD1B54A32D192ED03ull;
  const unsigned t0 = __builtin_amdgcn_readfirstlane((unsigned)tg), t1 = __builtin_amdgcn_readfirstlane((unsigned)(tg >> 32));
  int full = 0, polls = POLL_BUDGET;
  auto wait_full = [&](int need) { wait_ge(lbase + C_FULL, need, full, budget); };
  auto wait_word = [&](int word, int need) { int seen = 0; wait_ge(lbase + word, need, seen, budget); };
  auto arrive = [&](int word) { CBAR(); *(volatile LAS unsigned char*)(L + word + wave) = 1; };
  auto counter = [&](int branch, int head) { return (GAS unsigned*)(p.ws + WS_CT + ((branch * 4 + g) * 8 + head) * 128); };

  if (wave == 2 && s < 16) {
    __hip_atomic_store(counter(s >> 3, s & 7), 0u, __ATOMIC_RELAXED, __HIP_MEMORY_SCOPE_AGENT);
    waitvm<0>();
  }
  const int qrec = (g * 16 + cg) * 4;
  {
    const unsigned wofs = (unsigned)((8 * c + q4) * 128 + ((wave ^ (((q4 >> 1) & 1) | ((c & 1) << 1))) << 5) + 8 * p4);
#pragma unroll 1
    for (int bb = 0; bb < 2; ++bb) {
      f32x4 acc = {0.f, 0.f, 0.f, 0.f};
#pragma unroll 1
      for (int f = 4 * bb; f < 4 * bb + 4; ++f) {
        wait_full(f + 1);
        LAS unsigned char* a = L + f * SLOTB + wofs;
        const v2u l0 = trread(a), h0 = trread(a + 512), l1 = trread(a + 4096), h1 = trread(a + 4096 + 512);
        const v4u x0 = *(LAS v4u*)(L + L_X + f * 128 + c * 16), x1 = *(LAS v4u*)(L + L_X + f * 128 + 64 + c * 16);
        acc = mfma(v4u{l0.x, l0.y, h0.x, h0.y}, x0, acc);
        acc = mfma(v4u{l1.x, l1.y, h1.x, h1.y}, x1, acc);
      }
      if (n < 2) stpair(rs, WS_QR + (bb * 256 + qrec + wave) * 512 + ksp * 128 + (2 * c + n) * 16, f2u(sel(n, acc.z, acc.x)), f2u(sel(n, acc.w, acc.y)), t0, t1);
      CBAR();
      prog[wave] = bb ? (unsigned)(FKV + 2 * wave) : 4u;
    }
  }
  auto q_reduce = [&](int bb) {
    v4u G;
#pragma unroll 1
    for (;;) {
      CBAR();
      G = ldsc1(rs, (lane & 31) * 16, WS_QR + (bb * 256 + qrec + ksp) * 512);
      if (__all(tagok(G, t0, t1)) || --polls <= 0) break;
    }
    float a0 = u2f(G.x), a1 = u2f(G.z);
    a0 += dpp<ROR8>(a0); a1 += dpp<ROR8>(a1);
    auto s0 = __builtin_amdgcn_permlane16_swap(f2u(a0), f2u(a0), false, false);
    auto s1 = __builtin_amdgcn_permlane16_swap(f2u(a1), f2u(a1), false, false);
    const unsigned pk = pack2(u2f(s0[0]) + u2f(s0[1]), u2f(s1[0]) + u2f(s1[1]));
    const unsigned pn = dppu<XOR1>(pk);
    if (lane < 8 && !(lane & 1)) stpair(rs, WS_QP + (bb * 4 + g) * 4096 + (cg * 16 + ksp * 4 + (lane >> 1)) * 16, pk, pn, t0, t1);
  };
  if (wave == 0) { wait_full(AT_QA); q_reduce(0); }
  const unsigned kofs = (unsigned)(n * 256 + ((c ^ n) << 4));
  const unsigned vofs = (unsigned)((4 * c + q4) * 256 + ((((c & 1) << 2) | q4) << 5) + 8 * p4);
  v4u Kf[2][4], Vf[8];
  auto read_k = [&](LAS unsigned char* sK, int fK) {
    wait_full(fK + 1);
#pragma unroll
    for (int k = 0; k < 4; ++k)
#pragma unroll
      for (int u = 0; u < 2; ++u) Kf[u][k] = *(LAS v4u*)(sK + ((kofs ^ (k << 6)) + u * 4096));
    CBAR();
    prog[wave] = fK + 1;
    CBAR();
    __builtin_amdgcn_sched_barrier(0);
  };
  auto read_v = [&](LAS unsigned char* sK, int fK) {
    wait_full(fK + 2);
#pragma unroll
    for (int e = 0; e < 8; ++e) {
      LAS unsigned char* a = sK + SLOTB + (vofs ^ (e << 5));
      const v2u lo = trread(a), hi = trread(a + 4096);
      Vf[e] = v4u{lo.x, lo.y, hi.x, hi.y};
    }
    CBAR();
    prog[wave] = fK + 2 * NCONS;
    CBAR();
    __builtin_amdgcn_sched_barrier(0);
  };
  read_k(L + (FKV + 2 * wave) * SLOTB, FKV + 2 * wave);
  read_v(L + (FKV + 2 * wave) * SLOTB, FKV + 2 * wave);
  if (wave == 1) { wait_full(AT_QB); q_reduce(1); }
  wait_full(AT_Q);
  {
    v4u G;
#pragma unroll 1
    for (;;) {
      CBAR();
      G = ldsc1(rs, lane * 16, WS_QP + g * 4096 + wave * 1024);
      if (__all(tagok(G, t0, t1)) || --polls <= 0) break;
    }
    *(LAS v2u*)(L + L_Q + (2 * wave + (lane >> 5)) * QSTR + (lane & 31) * 8) = v2u{G.x, G.z};
    arrive(C_Q);
  }
  wait_word(C_Q, ALL4);
  v4u Qf[4];
  auto read_q = [&](int bb) {
#pragma unroll
    for (int k = 0; k < 4; ++k) Qf[k] = *(LAS v4u*)(L + L_Q + (8 * bb + (n & 7)) * QSTR + (4 * k + c) * 16);
  };
  read_q(0);

  float m = -INFINITY, l = 0.f;
  f32x4 O[8];
#pragma unroll
  for (int e = 0; e < 8; ++e) O[e] = f32x4{0.f, 0.f, 0.f, 0.f};
  auto softmax_tile = [&]() {
    f32x4 S0 = {0.f, 0.f, 0.f, 0.f}, S1 = {0.f, 0.f, 0.f, 0.f};
#pragma unroll
    for (int k = 0; k < 4; ++k) {
      S0 = mfma(Kf[0][k], Qf[k], S0);
      S1 = mfma(Kf[1][k], Qf[k], S1);
    }
    const float tm = xrow_max(fmx(fmx(fmx(S0.x, S0.y), fmx(S0.z, S0.w)), fmx(fmx(S1.x, S1.y), fmx(S1.z, S1.w))));
    if (__any(tm > m)) {
      const float mn = fmx(m, tm), al = __builtin_amdgcn_exp2f((m - mn) * C2);
      l *= al;
#pragma unroll
      for (int e = 0; e < 8; ++e) O[e] *= al;
      m = mn;
    }
    const float mb = -m * C2;
    float pp[8];
#pragma unroll
    for (int i = 0; i < 4; ++i) {
      pp[i] = __builtin_amdgcn_exp2f(fmaf(S0[i], C2, mb));
      pp[4 + i] = __builtin_amdgcn_exp2f(fmaf(S1[i], C2, mb));
    }
    l += ((pp[0] + pp[1]) + (pp[2] + pp[3])) + ((pp[4] + pp[5]) + (pp[6] + pp[7]));
    v4u Pf = {pack2(pp[0], pp[1]), pack2(pp[2], pp[3]), pack2(pp[4], pp[5]), pack2(pp[6], pp[7])};
    asm volatile("" : "+v"(Pf), "+v"(l));
    return Pf;
  };
  {
    const v4u Pf = softmax_tile();
#pragma unroll
    for (int e = 0; e < 8; ++e) O[e] = mfma(Vf[e], Pf, O[e]);
  }
  int slot = FKV + 2 * wave;
  auto run = [&](int f0, int f1, int step) {
#pragma unroll 1
    for (int fK = f0; fK < f1; fK += step) {
      LAS unsigned char* sK = L + slot * SLOTB;
      read_k(sK, fK);
      const v4u Pf = softmax_tile();
      read_v(sK, fK);
#pragma unroll
      for (int e = 0; e < 8; ++e) O[e] = mfma(Vf[e], Pf, O[e]);
      slot = slot + step >= NSLOT ? slot + step - NSLOT : slot + step;
    }
  };
  slot = slot + 2 * NCONS >= NSLOT ? slot + 2 * NCONS - NSLOT : slot + 2 * NCONS;
  run(FKV + 2 * (wave + NCONS), FKV + 2 * NCONS * R_BEACON, 2 * NCONS);
  unsigned tb = 0;
  if (wave == 2) {
    tb = (unsigned)__builtin_amdgcn_s_memrealtime();
    if (lane == 0) stpair(rs, WS_BC + b * 16, tb, ts, t0, t1);
  }
  run(FKV + 2 * (wave + NCONS * R_BEACON), FKV + 2 * NCONS * R_STEER, 2 * NCONS);
  if (wave == 2) {
    const v4u G = ldsc1(rs, lane * 16, WS_BC + g * 1024);
    const bool ok = tagok(G, t0, t1);
    float d = ok ? (float)(int)(tb - G.x) - 0.375f * (float)(int)(ts - G.z) : 0.f, cnt = ok ? 1.f : 0.f;
    d += dpp<XOR1>(d); d += dpp<XOR2>(d); d += dpp<ROR4>(d); d += dpp<ROR8>(d); d = xrow_sum(d);
    cnt += dpp<XOR1>(cnt); cnt += dpp<XOR2>(cnt); cnt += dpp<ROR4>(cnt); cnt += dpp<ROR8>(cnt); cnt = xrow_sum(cnt);
    const int lag = (int)(d * __builtin_amdgcn_rcpf(cnt));
    constexpr int fnow = FKV + 2 * NCONS * R_STEER + 8;
    int st = 0;
    if (lag > 20) st = fnow + ((lag * 4) >> 4);
    else if (lag < -20) st = -(fnow + ((-lag * 4) >> 4));
    *(volatile LAS int*)(L + C_B) = st;
  }
  if (wave == 0) {
    v4u G[4];
#pragma unroll 1
    for (;;) {
      CBAR();
      bool ok = true;
#pragma unroll
      for (int j = 0; j < 4; ++j) G[j] = ldsc1(rs, lane * 16 + j * 1024, WS_QP + (4 + g) * 4096);
#pragma unroll
      for (int j = 0; j < 4; ++j) ok &= tagok(G[j], t0, t1);
      if (__all(ok) || --polls <= 0) break;
    }
#pragma unroll
    for (int j = 0; j < 4; ++j) *(LAS v2u*)(L + L_Q + (8 + 2 * j + (lane >> 5)) * QSTR + (lane & 31) * 8) = v2u{G[j].x, G[j].z};
    CBAR();
  }
  run(FKV + 2 * (wave + NCONS * R_STEER), FB, 2 * NCONS);

  constexpr int DEP[4] = {L_DEP, ((FB - 6) % NSLOT) * SLOTB, ((FB - 4) % NSLOT) * SLOTB, ((FB - 2) % NSLOT) * SLOTB};
  static_assert(FB - 6 > NFILL - 1 - NSLOT, "no later fill reuses a deposit slot");
  {
    const float lt = xrow_sum(l);
    if (n < 8) {
      LAS unsigned char* d = L + (wave == 0 ? DEP[0] : wave == 1 ? DEP[1] : wave == 2 ? DEP[2] : DEP[3]) + n * DSTR + c * 16;
#pragma unroll
      for (int e = 0; e < 8; ++e) *(LAS f32x4*)(d + e * 64) = O[e];
      if (c == 0) *(LAS f32x2*)(d + 512) = f32x2{m * C2, lt};
    }
    arrive(C_D);
  }
  if (wave >= 2) {
    const int e = ((lane >> 2) & 6) | (lane & 1), cc = (lane >> 1) & 3;
    float M[2], Ls[2];
    unsigned cs[2];
    wait_word(C_D, ALL4);
#pragma unroll
    for (int j = 0; j < 2; ++j) {
      const int h = 4 * (wave - 2) + 2 * j + (lane >> 5);
      LAS unsigned char* r = L + h * DSTR;
      f32x4 P[4];
      f32x2 ml[4];
#pragma unroll
      for (int w = 0; w < 4; ++w) {
        ml[w] = *(LAS f32x2*)(r + DEP[w] + 512);
        P[w] = *(LAS f32x4*)(r + DEP[w] + e * 64 + cc * 16);
      }
      M[j] = fmx(fmx(ml[0].x, ml[1].x), fmx(ml[2].x, ml[3].x));
      float wt[4];
#pragma unroll
      for (int w = 0; w < 4; ++w) wt[w] = __builtin_amdgcn_exp2f(ml[w].x - M[j]);
      Ls[j] = (wt[0] * ml[0].y + wt[1] * ml[1].y) + (wt[2] * ml[2].y + wt[3] * ml[3].y);
      const f32x4 o = (P[0] * wt[0] + P[1] * wt[1]) + (P[2] * wt[2] + P[3] * wt[3]);
      const unsigned a0 = pack2(o.x, o.y), a1 = pack2(o.z, o.w);
      const v4u d = {a0, a1, dppu<XOR1>(a0), dppu<XOR1>(a1)};
      if (!(lane & 1)) stsc1(rs, WS_PART + (g * 8 + h) * 16384 + ((lane >> 3) & 3) * 4096 + s * 64 + (lane & 7) * 8, d);
      cs[j] = (d.x + d.y) + (d.z + d.w);
      cs[j] += dppu<XOR2>(cs[j]);
      cs[j] += dppu<HMIR>(cs[j]);
    }
    const int j = lane & 1, h = 4 * (wave - 2) + 2 * j + (lane >> 5), rec = WS_ML + ((g * 8 + h) * 64 + s) * 32;
    if (!(lane & 6)) __builtin_amdgcn_raw_buffer_store_b32((j ? cs[1] : cs[0]) + t0, rs, rec + 16 + ((lane >> 3) & 3) * 4, 0, SC1);
    if (!(lane & 30)) {
      stpair(rs, rec, f2u(sel(j, M[1], M[0])), f2u(sel(j, Ls[1], Ls[0])), t0, t1);
      __hip_atomic_fetch_add(counter(0, h), 1u, __ATOMIC_RELAXED, __HIP_MEMORY_SCOPE_AGENT);
    }
  } else if (wave == 0) {
    m = -INFINITY;
    l = 0.f;
#pragma unroll
    for (int e = 0; e < 8; ++e) O[e] = f32x4{0.f, 0.f, 0.f, 0.f};
    read_q(1);
    slot = FB % NSLOT;
    run(FB, FB + 2, 2);
    LAS unsigned char* sK = L + slot * SLOTB;
    read_k(sK, FB + 2);
    const v4u Pf = softmax_tile();
    float lt = xrow_sum(l), mc = m * C2;
    int ob = WS_PART + ((4 + g) * 8 + (n & 7)) * 16384 + (n >> 3) * 4096 + s * 64 + c * 16;
    int oc = WS_ML + ((4 + g) * 512 + s) * 32 + (n & 7) * 2048;
    asm volatile("" : "+v"(lt), "+v"(mc), "+v"(ob), "+v"(oc));
    read_v(sK, FB + 2);
    if (lane < 8) __hip_atomic_fetch_add(counter(1, lane), 1u, __ATOMIC_RELAXED, __HIP_MEMORY_SCOPE_AGENT);
#pragma unroll
    for (int e = 0; e < 8; ++e) O[e] = mfma(Vf[e], Pf, O[e]);
    unsigned cs[2];
#pragma unroll
    for (int i = 0; i < 2; ++i) {
      float v[8];
#pragma unroll
      for (int k = 0; k < 4; ++k) {
        v[k] = sel(n < 8, O[4 * i][k], O[4 * i + 2][k]);
        v[4 + k] = sel(n < 8, O[4 * i + 1][k], O[4 * i + 3][k]);
      }
      const v4u d = {pack2(v[0], v[1]), pack2(v[2], v[3]), pack2(v[4], v[5]), pack2(v[6], v[7])};
      stsc1(rs, ob + i * 8192, d);
      cs[i] = (d.x + d.y) + (d.z + d.w);
    }
    {
      auto x = __builtin_amdgcn_permlane32_swap(cs[0], cs[1], false, false);
      unsigned y = x[0] + x[1];
      auto z = __builtin_amdgcn_permlane16_swap(y, y, false, false);
      y = z[0] + z[1];
      if (!(lane & 16)) __builtin_amdgcn_raw_buffer_store_b32(y + t0, rs, oc + 16 + ((lane >> 5) * 2 + (n >> 3)) * 4, 0, SC1);
    }
    if (lane < 8) stpair(rs, oc, f2u(mc), f2u(lt), t0, t1);
  }

  {
    int om = WS_ML + ((br * 4 + g) * 8 + hh) * 64 * 32;
    int od = WS_PART + ((br * 4 + g) * 8 + hh) * 16384 + wave * 4096;
    int vd = lane * 16, vm = lane * 32;
    int oo = WS_OP + ((br * 4 + ksp) * 32 + g * 8 + hh) * 2048 + wave * 512 + lane * 8;
    unsigned aw = lbase + L_W + (lane >> 2) * 4;
    unsigned ao = lbase + L_O + wave * 64 + p4 * 16 + c * 4;
    asm volatile("" : "+v"(vd), "+v"(vm), "+v"(oo), "+v"(aw), "+v"(ao), "+s"(om), "+s"(od));
    v4u Wf[16];
    const unsigned wofs = (unsigned)((4 * c + q4) * 512 + 8 * p4), h3 = (unsigned)(q4 | ((c & 1) << 2));
    auto stage = [&](auto ksc) {
      constexpr int ks = decltype(ksc)::value;
      wait_full(FWO + 2 * ks + 2);
      LAS unsigned char* b0 = L + ((FWO + 2 * ks) % NSLOT) * SLOTB + wofs;
#pragma unroll
      for (int k = 0; k < 4; ++k) {
        const unsigned blk = (unsigned)(4 * wave + k), o = (blk >> 3) * 256 + (((blk & 7) ^ h3) << 5);
        const v2u lo = trread(b0 + o), hi = trread(b0 + SLOTB + o);
        Wf[4 * ks + k] = v4u{lo.x, lo.y, hi.x, hi.y};
      }
      __builtin_amdgcn_sched_barrier(0);
    };
    if (wave == 1) {
      wait_full(AT_REC);
      int v, left = HINT_POLLS;
      asm volatile("1:\n\ts_load_dword %0, %2, 0x0 glc\n\ts_waitcnt lgkmcnt(0)\n\ts_cmp_ge_u32 %0, 64\n\ts_cbranch_scc1 2f\n\t"
                   "s_sub_i32 %1, %1, 1\n\ts_cmp_gt_i32 %1, 0\n\ts_sleep 3\n\ts_cbranch_scc1 1b\n2:"
                   : "=&s"(v), "+s"(left) : "s"(counter(br, hh)) : "memory", "scc");
      *(volatile LAS unsigned*)(L + C_M) = 1u;
    } else {
      wait_word(C_M, 1);
    }
    v4u mlg = {0u, 0u, 0u, 0u}, csg = {0u, 0u, 0u, 0u}, D[4];
    if (wave == 1) { mlg = ldsc1(rs, vm, om); csg = ldsc1(rs, vm + 16, om); }
#pragma unroll
    for (int i = 0; i < 4; ++i) D[i] = ldsc1(rs, vd + i * 1024, od);
    __builtin_amdgcn_sched_barrier(0);
    stage(IC<0>{});
    stage(IC<1>{});
    if (wave == 1) {
      if (!__all(tagok(mlg, t0, t1))) {
        bool ok;
#pragma unroll 1
        do {
          CBAR();
          mlg = ldsc1(rs, vm, om);
          csg = ldsc1(rs, vm + 16, om);
          asm volatile("" : "+v"(csg));
          ok = __all(tagok(mlg, t0, t1));
        } while (!ok && --polls > 0);
      }
#pragma unroll
      for (int k = 0; k < 4; ++k) *(LAS unsigned*)(L + L_CS + k * 256 + lane * 4) = csg[k];
      const float ms = u2f(mlg.x);
      float M = ms;
      M = fmx(M, dpp<XOR1>(M)); M = fmx(M, dpp<XOR2>(M)); M = fmx(M, dpp<ROR4>(M)); M = fmx(M, dpp<ROR8>(M));
      M = xrow_max(M);
      const float wl = __builtin_amdgcn_exp2f(ms - M);
      *(LAS float*)(L + L_W + lane * 4) = wl;
      CBAR();
      *(volatile LAS unsigned*)(L + C_W) = 1u;
      float Ls = wl * u2f(mlg.z);
      Ls += dpp<XOR1>(Ls); Ls += dpp<XOR2>(Ls); Ls += dpp<ROR4>(Ls); Ls += dpp<ROR8>(Ls);
      *(LAS float*)(L + L_W + 256) = __builtin_amdgcn_rcpf(xrow_sum(Ls));
      CBAR();
      *(volatile LAS unsigned*)(L + C_W) = 2u;
    }
    f32x2 u[4][4];
    unsigned sum[4], ck[4];
    auto unpack = [&]() {
#pragma unroll
      for (int i = 0; i < 4; ++i) {
#pragma unroll
        for (int k = 0; k < 4; ++k) u[i][k] = f32x2{u2f(D[i][k] << 16), u2f(D[i][k] & 0xffff0000u)};
        sum[i] = (D[i].x + D[i].y) + (D[i].z + D[i].w);
      }
#pragma unroll
      for (int i = 0; i < 4; ++i) sum[i] += dppu<XOR1>(sum[i]);
#pragma unroll
      for (int i = 0; i < 4; ++i) sum[i] += dppu<XOR2>(sum[i]) + t0;
    };
    auto intact = [&]() { return (bool)__all((((sum[0] ^ ck[0]) | (sum[1] ^ ck[1])) | ((sum[2] ^ ck[2]) | (sum[3] ^ ck[3]))) == 0u); };
    unpack();
#pragma unroll
    for (int i = 0; i < 4; ++i) asm volatile("" : "+v"(u[i][0]), "+v"(u[i][1]), "+v"(u[i][2]), "+v"(u[i][3]), "+v"(sum[i]));
    int wseen = 0;
    wait_ge(lbase + C_W, 1, wseen, budget);
    float wi[4];
#pragma unroll
    for (int i = 0; i < 4; ++i) {
      wi[i] = *(LAS float*)(uintptr_t)(aw + i * 64);
      ck[i] = *(LAS unsigned*)(uintptr_t)(aw + (L_CS - L_W) + wave * 256 + i * 64);
    }
    if (!intact()) {
      bool ok;
#pragma unroll 1
      do {
        CBAR();
#pragma unroll
        for (int i = 0; i < 4; ++i) {
          ck[i] = __builtin_amdgcn_raw_buffer_load_b32(rs, (lane >> 2) * 32 + 16 + wave * 4, om + i * 512, SC1);
          D[i] = ldsc1(rs, vd + i * 1024, od);
        }
        unpack();
        ok = intact();
      } while (!ok && --polls > 0);
    }
    f32x2 a[4];
#pragma unroll
    for (int k = 0; k < 4; ++k) a[k] = (u[0][k] * wi[0] + u[1][k] * wi[1]) + (u[2][k] * wi[2] + u[3][k] * wi[3]);
    float h4[4], h2[2];
#pragma unroll
    for (int k = 0; k < 4; ++k) {
      auto x = __builtin_amdgcn_permlane32_swap(f2u(a[k >> 1][k & 1]), f2u(a[2 + (k >> 1)][k & 1]), false, false);
      h4[k] = u2f(x[0]) + u2f(x[1]);
    }
#pragma unroll
    for (int k = 0; k < 2; ++k) {
      auto x = __builtin_amdgcn_permlane16_swap(f2u(h4[k]), f2u(h4[2 + k]), false, false);
      h2[k] = u2f(x[0]) + u2f(x[1]);
      h2[k] += dpp<ROR4>(h2[k]);
      h2[k] += dpp<ROR8>(h2[k]);
    }
    asm volatile("" : "+v"(h2[0]), "+v"(h2[1]));
    wait_ge(lbase + C_W, 2, wseen, budget);
    const float inv = *(LAS float*)(L + L_W + 256);
    *(LAS unsigned*)(uintptr_t)ao = pack2(h2[0] * inv, h2[1] * inv);
    arrive(C_O);
    wait_word(C_O, ALL4);
    f32x4 d[4];
#pragma unroll
    for (int k = 0; k < 4; ++k) d[k] = f32x4{0.f, 0.f, 0.f, 0.f};
    v4u Of[4];
#pragma unroll
    for (int ks = 0; ks < 4; ++ks) Of[ks] = *(LAS v4u*)(L + L_O + ks * 64 + c * 16);
    auto kstep = [&](int ks) {
#pragma unroll
      for (int k = 0; k < 4; ++k) d[k] = mfma(Of[ks], Wf[4 * ks + k], d[k]);
    };
    stage(IC<2>{});
    kstep(0);
    kstep(1);
    __builtin_amdgcn_sched_barrier(0);
    stage(IC<3>{});
    kstep(2);
    kstep(3);
    const float r = sel(c & 2, sel(c & 1, d[3].x, d[2].x), sel(c & 1, d[1].x, d[0].x));
    const float rn = dpp<XOR1>(r);
    if (!(lane & 1)) stpair(rs, oo, f2u(r), f2u(rn), t0, t1);
  }
  if (wave != 0) return;

  {
    const int j2 = ((b >> 3) & 1) | ((b & 7) << 1) | (b & 16), set = b >> 5;
    const int of = WS_OP + set * 32 * 2048 + j2 * 64, vf = (lane >> 2) * 2048 + p4 * 16;
    unsigned* po = p.out + set * 128 + j2 * 4 + (lane & 3);
    asm volatile("" : "+v"(po));
    v4u F[2];
    __builtin_amdgcn_s_sleep(16);
#pragma unroll 1
    for (;;) {
      CBAR();
      F[0] = ldsc1(rs, vf, of);
      F[1] = ldsc1(rs, vf, of + 16 * 2048);
      if (__all(tagok(F[0], t0, t1) && tagok(F[1], t0, t1)) || --polls <= 0) break;
    }
    float a0 = u2f(F[0].x) + u2f(F[1].x), a1 = u2f(F[0].z) + u2f(F[1].z);
    a0 += dpp<ROR4>(a0); a1 += dpp<ROR4>(a1);
    a0 += dpp<ROR8>(a0); a1 += dpp<ROR8>(a1);
    a0 = xrow_sum(a0); a1 = xrow_sum(a1);
    if (lane < 4) *(GAS unsigned*)po = pack2(a0, a1);
    if (b == 0 && lane == 0) __hip_atomic_store((GAS unsigned long long*)p.ws, tg, __ATOMIC_RELAXED, __HIP_MEMORY_SCOPE_AGENT);
  }
}
}

extern "C" void kernel_launch(void* const* d_in, const int* in_sizes, int n_in, void* d_out, int out_size, void* d_ws, size_t ws_size,
                              hipStream_t stream) {
  (void)in_sizes; (void)n_in; (void)out_size; (void)ws_size;
  fa::Params p;
  p.xA = (const unsigned char*)d_in[0]; p.kA = (const unsigned char*)d_in[1]; p.vA = (const unsigned char*)d_in[2];
  p.wqA = (const unsigned char*)d_in[3]; p.woA = (const unsigned char*)d_in[4];
  p.xB = (const unsigned char*)d_in[5]; p.kB = (const unsigned char*)d_in[6]; p.vB = (const unsigned char*)d_in[7];
  p.wqB = (const unsigned char*)d_in[8]; p.woB = (const unsigned char*)d_in[9];
  p.out = (unsigned*)d_out;
  p.ws = (unsigned char*)d_ws;
  fa::fused<<<fa::NWG, fa::NTHR, 0, stream>>>(p);
}
```

```cpp
#include <hip/hip_runtime.h>
#include <hip/hip_bf16.h>
#include <stdint.h>

#define GAS __attribute__((address_space(1)))
#define LAS __attribute__((address_space(3)))

namespace fa {
typedef unsigned v4u __attribute__((ext_vector_type(4)));
typedef unsigned v2u __attribute__((ext_vector_type(2)));
typedef float f32x4 __attribute__((ext_vector_type(4)));
typedef float f32x2 __attribute__((ext_vector_type(2)));
typedef short s16x4 __attribute__((ext_vector_type(4)));
typedef __bf16 bf16x8 __attribute__((ext_vector_type(8)));
typedef __bf16 bf16x2 __attribute__((ext_vector_type(2)));

constexpr int SK = 65536, WINB = 4096;
constexpr int NWG = 256, NCONS = 4, NTHR = (NCONS + 1) * 64;
constexpr int NSLOT = 18, SLOTB = 8192;
constexpr int INFL = 2, WIDE = 3;
constexpr int NTA = 32;
constexpr int FKV = 8, FB = FKV + 2 * NTA, FWO = FB + 4, NFILL = FWO + 8;
static_assert(FKV % 4 == 0 && FB % 4 == 0 && NSLOT % 2 == 0, "tiles come in pairs, K and V of a tile in neighbouring slots");
constexpr int AT_QA = 8, AT_QB = 13, AT_Q = 19, AT_REC = NFILL - 6;
constexpr int R_BEACON = 4, R_STEER = 5;
constexpr float C2 = 0.08838834764831845f * 1.4426950408889634f;

constexpr int QSTR = 272, DSTR = 528;
constexpr int L_Q = NSLOT * SLOTB, L_X = L_Q + 2 * 8 * QSTR, L_O = L_X + 1024, L_W = L_O + 256, L_CS = L_W + 272, L_DEP = L_CS + 1024,
              L_CTL = L_DEP + 8 * DSTR, L_END = L_CTL + 48;
static_assert(L_END <= 160 * 1024 && (L_X % 16) == 0 && (L_O % 16) == 0 && (L_DEP % 16) == 0 && (L_CTL % 16) == 0, "lds map");
constexpr int C_FULL = L_CTL + 16, C_M = L_CTL + 20, C_W = L_CTL + 24, C_Q = L_CTL + 28, C_D = L_CTL + 32, C_O = L_CTL + 36, C_B = L_CTL + 40,
              ALL4 = 0x01010101;
constexpr int WS_QR = 4096, WS_QP = WS_QR + 2 * 256 * 512, WS_ML = WS_QP + 2 * 4 * 4096, WS_PART = WS_ML + 2 * 4 * 8 * 64 * 32,
              WS_OP = WS_PART + 2 * 4 * 8 * 64 * 256, WS_BC = WS_OP + 2 * 4 * 32 * 2048, WS_CT = WS_BC + 4 * 1024, WS_END = WS_CT + 64 * 128;
constexpr int SC1 = 16;
constexpr int SPIN_BUDGET = 1 << 22, POLL_BUDGET = 1 << 19;
constexpr int HINT_POLLS = 1 << 14;

struct Params {
  const unsigned char *wqA, *wqB, *xA, *xB, *kA, *vA, *kB, *vB, *woA, *woB;
  unsigned* out;
  unsigned char* ws;
};

template <int V> struct IC { static constexpr int value = V; };
#define CBAR() asm volatile("" ::: "memory")
template <int N> __device__ __forceinline__ void waitvm() { asm volatile("s_waitcnt vmcnt(%0)" ::"i"(N) : "memory"); }
__device__ __forceinline__ void fill8(const unsigned char* ubase, const unsigned (&vo)[8], unsigned dst) {
  asm volatile(
      "s_mov_b32 m0, %9\n\ts_nop 4\n\t"
      "global_load_lds_dwordx4 %0, %8 nt\n\tglobal_load_lds_dwordx4 %1, %8 offset:1024 nt\n\t"
      "global_load_lds_dwordx4 %2, %8 offset:2048 nt\n\tglobal_load_lds_dwordx4 %3, %8 offset:3072 nt\n\t"
      "s_or_b32 m0, %9, 0x1000\n\ts_nop 0\n\t"
      "global_load_lds_dwordx4 %4, %8 nt\n\tglobal_load_lds_dwordx4 %5, %8 offset:1024 nt\n\t"
      "global_load_lds_dwordx4 %6, %8 offset:2048 nt\n\tglobal_load_lds_dwordx4 %7, %8 offset:3072 nt"
      ::"v"(vo[0]), "v"(vo[1]), "v"(vo[2]), "v"(vo[3]), "v"(vo[4]), "v"(vo[5]), "v"(vo[6]), "v"(vo[7]), "s"(ubase), "s"(dst)
      : "memory", "scc");
}
__device__ __forceinline__ f32x4 mfma(v4u a, v4u b, f32x4 c) {
  return __builtin_amdgcn_mfma_f32_16x16x32_bf16(__builtin_bit_cast(bf16x8, a), __builtin_bit_cast(bf16x8, b), c, 0, 0, 0);
}
__device__ __forceinline__ v2u trread(LAS unsigned char* p) {
  return __builtin_bit_cast(v2u, __builtin_amdgcn_ds_read_tr16_b64_v4i16((LAS s16x4*)p));
}
__device__ __forceinline__ unsigned pack2(float a, float b) {
  f32x2 t = {a, b};
  return __builtin_bit_cast(unsigned, __builtin_convertvector(t, bf16x2));
}
__device__ __forceinline__ float u2f(unsigned x) { return __builtin_bit_cast(float, x); }
__device__ __forceinline__ unsigned f2u(float x) { return __builtin_bit_cast(unsigned, x); }
__device__ __forceinline__ float fmx(float a, float b) { return __builtin_elementwise_maximum(a, b); }
template <int CTRL> __device__ __forceinline__ unsigned dppu(unsigned x) { return (unsigned)__builtin_amdgcn_mov_dpp((int)x, CTRL, 0xf, 0xf, true); }
template <int CTRL> __device__ __forceinline__ float dpp(float x) { return u2f(dppu<CTRL>(f2u(x))); }
constexpr int HMIR = 0x141, XOR1 = 0xB1, XOR2 = 0x4E, ROR4 = 0x124, ROR8 = 0x128;
__device__ __forceinline__ float xrow_max(float x) {
  auto s = __builtin_amdgcn_permlane16_swap(f2u(x), f2u(x), false, false);
  x = fmx(u2f(s[0]), u2f(s[1]));
  auto t = __builtin_amdgcn_permlane32_swap(f2u(x), f2u(x), false, false);
  return fmx(u2f(t[0]), u2f(t[1]));
}
__device__ __forceinline__ float xrow_sum(float x) {
  auto s = __builtin_amdgcn_permlane16_swap(f2u(x), f2u(x), false, false);
  x = u2f(s[0]) + u2f(s[1]);
  auto t = __builtin_amdgcn_permlane32_swap(f2u(x), f2u(x), false, false);
  return u2f(t[0]) + u2f(t[1]);
}
__device__ __forceinline__ v4u ldsc1(__amdgpu_buffer_rsrc_t rs, int voff, int soff) { return __builtin_amdgcn_raw_buffer_load_b128(rs, voff, soff, SC1); }
__device__ __forceinline__ void stsc1(__amdgpu_buffer_rsrc_t rs, int off, v4u d) { __builtin_amdgcn_raw_buffer_store_b128(d, rs, off, 0, SC1); }
__device__ __forceinline__ void stpair(__amdgpu_buffer_rsrc_t rs, int off, unsigned a, unsigned b, unsigned t0, unsigned t1) { stsc1(rs, off, v4u{a, t0, b, t1}); }
__device__ __forceinline__ float sel(bool c, float a, float b) {
  asm("" : "+v"(a));
  return c ? a : b;
}
__device__ __forceinline__ bool tagok(v4u g, unsigned t0, unsigned t1) { return g.y == t0 && g.w == t1; }

__device__ __forceinline__ void wait_ge(unsigned a, int need, int& seen, int& budget) {
  unsigned v;
  seen = __builtin_amdgcn_readfirstlane(seen);
  budget = __builtin_amdgcn_readfirstlane(budget);
  need = __builtin_amdgcn_readfirstlane(need);
  asm volatile(
      "s_cmp_ge_i32 %0, %4\n\ts_cbranch_scc1 2f\n"
      "1:\n\tds_read_b32 %2, %3\n\ts_waitcnt lgkmcnt(0)\n\tv_readfirstlane_b32 %0, %2\n\t"
      "s_cmp_ge_i32 %0, %4\n\ts_cbranch_scc1 2f\n\t"
      "s_sub_i32 %1, %1, 1\n\ts_cmp_gt_i32 %1, 0\n\ts_cbranch_scc1 1b\n"
      "2:"
      : "+s"(seen), "+s"(budget), "=&v"(v) : "v"(a), "s"(need) : "memory", "scc");
}

__global__ void __launch_bounds__(NTHR, 2) fused(Params p) {
  __shared__ __attribute__((aligned(8192))) unsigned char smem[L_END];
  LAS unsigned char* L = (LAS unsigned char*)smem;
  const unsigned lbase = (unsigned)(uintptr_t)L;
  volatile LAS unsigned* prog = (volatile LAS unsigned*)(L + L_CTL);
  const int tid = threadIdx.x, lane = tid & 63, wave = __builtin_amdgcn_readfirstlane(tid >> 6);
  const int b = blockIdx.x, g = b >> 6, s = b & 63;
  const int cg = (s & 7) | ((s >> 5) << 3), ksp = (s >> 3) & 3, br = s >> 5 ? 0 : 1, hh = s & 7;
  int budget = SPIN_BUDGET;

  if (!(wave < NCONS)) {
    __builtin_amdgcn_s_setprio(3);
    unsigned vk[8], vv[8], vw[8];
#pragma unroll
    for (int i = 0; i < 8; ++i)
      vw[i] = (unsigned)(lane >> 3) * 8192u + ((unsigned)(((lane & 7) ^ (((lane >> 4) & 1) << 1)) << 4) ^ ((i & 1) << 6)) + i * 65536u - (i & 3) * 1024u;
    const size_t qoff = ((size_t)ksp * 256 * 4096 + g * 1024 + cg * 64) * 2;
    int pub = 0;
    auto publish = [&](int cnt) { pub = max(pub, cnt); prog[4] = (unsigned)pub; };
#pragma unroll
    for (int f = 0; f < FKV; ++f) {
      fill8((f < 4 ? p.wqA : p.wqB) + qoff + (size_t)(f & 3) * 64 * 8192, vw, lbase + f * SLOTB);
      if (f == 0) {
        const unsigned char* xs = (lane < 32 ? p.xA : p.xB) + ksp * 512 + (lane & 31) * 16;
        asm volatile("s_mov_b32 m0, %1\n\ts_nop 0\n\tglobal_load_lds_dwordx4 %0, off" ::"v"(xs), "s"(lbase + L_X) : "memory");
        if (lane < 12) prog[lane] = 0u;
      }
      if (f == INFL - 1) {
        asm volatile("s_waitcnt lgkmcnt(0)" ::: "memory");
        __builtin_amdgcn_s_barrier();
      }
      if (f >= WIDE) { waitvm<8 * WIDE>(); publish(f + 1 - WIDE); }
    }
#pragma unroll
    for (int i = 0; i < 8; ++i) {
      vk[i] = (unsigned)(lane >> 4) * 256u + ((unsigned)(((lane & 15) ^ (lane >> 4)) << 4) ^ ((i & 3) << 6)) + (i >> 2) * 8192u;
      vv[i] = (unsigned)(lane >> 4) * 256u + ((unsigned)(((lane & 15) ^ ((lane >> 4) << 1)) << 4) ^ ((i & 1) << 7)) + (i >> 2) * 8192u;
    }
    auto minprog = [&]() {
      const v4u pr = *(volatile LAS v4u*)(L + L_CTL);
      return (int)__builtin_amdgcn_readfirstlane(min(min(pr.x, pr.y), min(pr.z, pr.w)));
    };
    auto park = [&](int f, int need) {
      static_assert(WIDE == 3 && INFL <= WIDE, "drain ladder");
      waitvm<16>(); publish(f - 2); if (minprog() > need) return;
      waitvm<8>(); publish(f - 1); if (minprog() > need) return;
      waitvm<0>(); publish(f);
#pragma unroll 1
      while (minprog() <= need && --budget > 0) __builtin_amdgcn_s_sleep(1);
    };
    int steer = 0;
    auto settle = [&](int f, int issued) {
      if (f < steer) { waitvm<8 * WIDE>(); publish(issued - WIDE); }
      else { waitvm<8 * INFL>(); publish(issued - INFL); if (f < -steer) __builtin_amdgcn_s_sleep(6); }
    };
    const int ca = ((s & 7) >> 1) * 16 + (s >> 3) * 2 + (s & 1), cb = (5 * s + 16 * g) & 63;
    const size_t aoff = ((size_t)g * SK + ca * 1024) * 256, boff = ((size_t)g * SK + (SK - WINB) + cb * 64) * 256;
    const unsigned char *kp = p.kA + aoff, *vp = p.vA + aoff;
    int mp = 0;
    unsigned dst = lbase + FKV * SLOTB;
    auto tiles = [&](int f0, int f1) {
#pragma unroll 1
      for (int f = f0; f < f1; f += 2) {
        if (mp <= f + 1 - NSLOT) park(f, f + 1 - NSLOT);
        fill8(kp, vk, dst);
        settle(f, f + 1);
        fill8(vp, vv, dst + SLOTB);
        const v4u pr = *(volatile LAS v4u*)(L + L_CTL);
        const int adv = (f & 2) ? 12288 : 4096;
        kp += adv; vp += adv;
        dst = dst + 2 * SLOTB == lbase + NSLOT * SLOTB ? lbase : dst + 2 * SLOTB;
        const unsigned st = *(volatile LAS unsigned*)(L + C_B);
        settle(f, f + 2);
        mp = (int)__builtin_amdgcn_readfirstlane(min(min(pr.x, pr.y), min(pr.z, pr.w)));
        steer = (int)__builtin_amdgcn_readfirstlane(st);
      }
    };
    tiles(FKV, FB);
#pragma unroll
    for (int i = 0; i < 8; ++i)
      vw[i] = (unsigned)(2 * i + (lane >> 5)) * 2048u + ((((unsigned)lane & 31u) ^ ((unsigned)((2 * i + (lane >> 5)) & 7) << 1)) << 4) - (i & 3) * 1024u;
    kp = p.kB + boff; vp = p.vB + boff;
    tiles(FB, FWO);
    const unsigned char* wo = (br ? p.woB : p.woA) + ((size_t)(g * 8 + hh) * 128 * 1024 + ksp * 256) * 2;
#pragma unroll 1
    for (int f = FWO; f < NFILL; ++f) {
      if (mp <= f - NSLOT) { park(f, f - NSLOT); mp = f - NSLOT + 1; }
      fill8(wo, vw, dst);
      const v4u pr = *(volatile LAS v4u*)(L + L_CTL);
      wo += 32768;
      dst = dst + SLOTB == lbase + NSLOT * SLOTB ? lbase : dst + SLOTB;
      settle(f, f + 1);
      mp = max(mp, (int)__builtin_amdgcn_readfirstlane(min(min(pr.x, pr.y), min(pr.z, pr.w))));
    }
    waitvm<16>(); publish(NFILL - 2);
    waitvm<8>(); publish(NFILL - 1);
    waitvm<0>(); publish(NFILL);
    return;
  }

  const __amdgpu_buffer_rsrc_t rs = __builtin_amdgcn_make_buffer_rsrc((void*)p.ws, 0, WS_END, 0x00020000);
  unsigned long long E = __hip_atomic_load((GAS unsigned long long*)p.ws, __ATOMIC_RELAXED, __HIP_MEMORY_SCOPE_AGENT);
  const int n = lane & 15, c = lane >> 4, q4 = (lane >> 2) & 3, p4 = lane & 3;
  const unsigned ts = (unsigned)__builtin_amdgcn_s_memrealtime();
  __builtin_amdgcn_s_barrier();
  asm volatile("" : "+v"(E)::"memory");
  const unsigned long long tg = E * 0x9E3779B97F4A7C15ull + 0xD1B54A32D192ED03ull;
  const unsigned t0 = __builtin_amdgcn_readfirstlane((unsigned)tg), t1 = __builtin_amdgcn_readfirstlane((unsigned)(tg >> 32));
  int full = 0, polls = POLL_BUDGET;
  auto wait_full = [&](int need) { wait_ge(lbase + C_FULL, need, full, budget); };
  auto wait_word = [&](int word, int need) { int seen = 0; wait_ge(lbase + word, need, seen, budget); };
  auto arrive = [&](int word) { CBAR(); *(volatile LAS unsigned char*)(L + word + wave) = 1; };
  auto counter = [&](int branch, int head) { return (GAS unsigned*)(p.ws + WS_CT + ((branch * 4 + g) * 8 + head) * 128); };

  if (wave == 2 && s < 16) {
    __hip_atomic_store(counter(s >> 3, s & 7), 0u, __ATOMIC_RELAXED, __HIP_MEMORY_SCOPE_AGENT);
    waitvm<0>();
  }
  const int qrec = (g * 16 + cg) * 4;
  {
    const unsigned wofs = (unsigned)((8 * c + q4) * 128 + ((wave ^ (((q4 >> 1) & 1) | ((c & 1) << 1))) << 5) + 8 * p4);
#pragma unroll 1
    for (int bb = 0; bb < 2; ++bb) {
      f32x4 acc = {0.f, 0.f, 0.f, 0.f};
#pragma unroll 1
      for (int f = 4 * bb; f < 4 * bb + 4; ++f) {
        wait_full(f + 1);
        LAS unsigned char* a = L + f * SLOTB + wofs;
        const v2u l0 = trread(a), h0 = trread(a + 512), l1 = trread(a + 4096), h1 = trread(a + 4096 + 512);
        const v4u x0 = *(LAS v4u*)(L + L_X + f * 128 + c * 16), x1 = *(LAS v4u*)(L + L_X + f * 128 + 64 + c * 16);
        acc = mfma(v4u{l0.x, l0.y, h0.x, h0.y}, x0, acc);
        acc = mfma(v4u{l1.x, l1.y, h1.x, h1.y}, x1, acc);
      }
      if (n < 2) stpair(rs, WS_QR + (bb * 256 + qrec + wave) * 512 + ksp * 128 + (2 * c + n) * 16, f2u(sel(n, acc.z, acc.x)), f2u(sel(n, acc.w, acc.y)), t0, t1);
      CBAR();
      prog[wave] = bb ? (unsigned)(FKV + 2 * wave) : 4u;
    }
  }
  auto q_reduce = [&](int bb) {
    v4u G;
#pragma unroll 1
    for (;;) {
      CBAR();
      G = ldsc1(rs, (lane & 31) * 16, WS_QR + (bb * 256 + qrec + ksp) * 512);
      if (__all(tagok(G, t0, t1)) || --polls <= 0) break;
    }
    float a0 = u2f(G.x), a1 = u2f(G.z);
    a0 += dpp<ROR8>(a0); a1 += dpp<ROR8>(a1);
    auto s0 = __builtin_amdgcn_permlane16_swap(f2u(a0), f2u(a0), false, false);
    auto s1 = __builtin_amdgcn_permlane16_swap(f2u(a1), f2u(a1), false, false);
    const unsigned pk = pack2(u2f(s0[0]) + u2f(s0[1]), u2f(s1[0]) + u2f(s1[1]));
    const unsigned pn = dppu<XOR1>(pk);
    if (lane < 8 && !(lane & 1)) stpair(rs, WS_QP + (bb * 4 + g) * 4096 + (cg * 16 + ksp * 4 + (lane >> 1)) * 16, pk, pn, t0, t1);
  };
  if (wave == 0) { wait_full(AT_QA); q_reduce(0); }
  const unsigned kofs = (unsigned)(n * 256 + ((c ^ n) << 4));
  const unsigned vofs = (unsigned)((4 * c + q4) * 256 + ((((c & 1) << 2) | q4) << 5) + 8 * p4);
  v4u Kf[2][4], Vf[8];
  auto read_k = [&](LAS unsigned char* sK, int fK) {
    wait_full(fK + 1);
#pragma unroll
    for (int k = 0; k < 4; ++k)
#pragma unroll
      for (int u = 0; u < 2; ++u) Kf[u][k] = *(LAS v4u*)(sK + ((kofs ^ (k << 6)) + u * 4096));
    CBAR();
    prog[wave] = fK + 1;
    CBAR();
    __builtin_amdgcn_sched_barrier(0);
  };
  auto read_v = [&](LAS unsigned char* sK, int fK) {
    wait_full(fK + 2);
#pragma unroll
    for (int e = 0; e < 8; ++e) {
      LAS unsigned char* a = sK + SLOTB + (vofs ^ (e << 5));
      const v2u lo = trread(a), hi = trread(a + 4096);
      Vf[e] = v4u{lo.x, lo.y, hi.x, hi.y};
    }
    CBAR();
    prog[wave] = fK + 2 * NCONS;
    CBAR();
    __builtin_amdgcn_sched_barrier(0);
  };
  read_k(L + (FKV + 2 * wave) * SLOTB, FKV + 2 * wave);
  read_v(L + (FKV + 2 * wave) * SLOTB, FKV + 2 * wave);
  if (wave == 1) { wait_full(AT_QB); q_reduce(1); }
  wait_full(AT_Q);
  {
    v4u G;
#pragma unroll 1
    for (;;) {
      CBAR();
      G = ldsc1(rs, lane * 16, WS_QP + g * 4096 + wave * 1024);
      if (__all(tagok(G, t0, t1)) || --polls <= 0) break;
    }
    *(LAS v2u*)(L + L_Q + (2 * wave + (lane >> 5)) * QSTR + (lane & 31) * 8) = v2u{G.x, G.z};
    arrive(C_Q);
  }
  wait_word(C_Q, ALL4);
  v4u Qf[4];
  auto read_q = [&](int bb) {
#pragma unroll
    for (int k = 0; k < 4; ++k) Qf[k] = *(LAS v4u*)(L + L_Q + (8 * bb + (n & 7)) * QSTR + (4 * k + c) * 16);
  };
  read_q(0);

  float m = -INFINITY, l = 0.f;
  f32x4 O[8];
#pragma unroll
  for (int e = 0; e < 8; ++e) O[e] = f32x4{0.f, 0.f, 0.f, 0.f};
  auto softmax_tile = [&]() {
    f32x4 S0 = {0.f, 0.f, 0.f, 0.f}, S1 = {0.f, 0.f, 0.f, 0.f};
#pragma unroll
    for (int k = 0; k < 4; ++k) {
      S0 = mfma(Kf[0][k], Qf[k], S0);
      S1 = mfma(Kf[1][k], Qf[k], S1);
    }
    const float tm = xrow_max(fmx(fmx(fmx(S0.x, S0.y), fmx(S0.z, S0.w)), fmx(fmx(S1.x, S1.y), fmx(S1.z, S1.w))));
    if (__any(tm > m)) {
      const float mn = fmx(m, tm), al = __builtin_amdgcn_exp2f((m - mn) * C2);
      l *= al;
#pragma unroll
      for (int e = 0; e < 8; ++e) O[e] *= al;
      m = mn;
    }
    const float mb = -m * C2;
    float pp[8];
#pragma unroll
    for (int i = 0; i < 4; ++i) {
      pp[i] = __builtin_amdgcn_exp2f(fmaf(S0[i], C2, mb));
      pp[4 + i] = __builtin_amdgcn_exp2f(fmaf(S1[i], C2, mb));
    }
    l += ((pp[0] + pp[1]) + (pp[2] + pp[3])) + ((pp[4] + pp[5]) + (pp[6] + pp[7]));
    v4u Pf = {pack2(pp[0], pp[1]), pack2(pp[2], pp[3]), pack2(pp[4], pp[5]), pack2(pp[6], pp[7])};
    asm volatile("" : "+v"(Pf), "+v"(l));
    return Pf;
  };
  {
    const v4u Pf = softmax_tile();
#pragma unroll
    for (int e = 0; e < 8; ++e) O[e] = mfma(Vf[e], Pf, O[e]);
  }
  int slot = FKV + 2 * wave;
  auto run = [&](int f0, int f1, int step) {
#pragma unroll 1
    for (int fK = f0; fK < f1; fK += step) {
      LAS unsigned char* sK = L + slot * SLOTB;
      read_k(sK, fK);
      const v4u Pf = softmax_tile();
      read_v(sK, fK);
#pragma unroll
      for (int e = 0; e < 8; ++e) O[e] = mfma(Vf[e], Pf, O[e]);
      slot = slot + step >= NSLOT ? slot + step - NSLOT : slot + step;
    }
  };
  slot = slot + 2 * NCONS >= NSLOT ? slot + 2 * NCONS - NSLOT : slot + 2 * NCONS;
  run(FKV + 2 * (wave + NCONS), FKV + 2 * NCONS * R_BEACON, 2 * NCONS);
  unsigned tb = 0;
  if (wave == 2) {
    tb = (unsigned)__builtin_amdgcn_s_memrealtime();
    if (lane == 0) stpair(rs, WS_BC + b * 16, tb, ts, t0, t1);
  }
  run(FKV + 2 * (wave + NCONS * R_BEACON), FKV + 2 * NCONS * R_STEER, 2 * NCONS);
  if (wave == 2) {
    const v4u G = ldsc1(rs, lane * 16, WS_BC + g * 1024);
    const bool ok = tagok(G, t0, t1);
    float d = ok ? (float)(int)(tb - G.x) - 0.375f * (float)(int)(ts - G.z) : 0.f, cnt = ok ? 1.f : 0.f;
    d += dpp<XOR1>(d); d += dpp<XOR2>(d); d += dpp<ROR4>(d); d += dpp<ROR8>(d); d = xrow_sum(d);
    cnt += dpp<XOR1>(cnt); cnt += dpp<XOR2>(cnt); cnt += dpp<ROR4>(cnt); cnt += dpp<ROR8>(cnt); cnt = xrow_sum(cnt);
    const int lag = (int)(d * __builtin_amdgcn_rcpf(cnt));
    constexpr int fnow = FKV + 2 * NCONS * R_STEER + 8;
    int st = 0;
    if (lag > 20) st = fnow + ((lag * 4) >> 4);
    else if (lag < -20) st = -(fnow + ((-lag * 4) >> 4));
    *(volatile LAS int*)(L + C_B) = st;
  }
  if (wave == 0) {
    v4u G[4];
#pragma unroll 1
    for (;;) {
      CBAR();
      bool ok = true;
#pragma unroll
      for (int j = 0; j < 4; ++j) G[j] = ldsc1(rs, lane * 16 + j * 1024, WS_QP + (4 + g) * 4096);
#pragma unroll
      for (int j = 0; j < 4; ++j) ok &= tagok(G[j], t0, t1);
      if (__all(ok) || --polls <= 0) break;
    }
#pragma unroll
    for (int j = 0; j < 4; ++j) *(LAS v2u*)(L + L_Q + (8 + 2 * j + (lane >> 5)) * QSTR + (lane & 31) * 8) = v2u{G[j].x, G[j].z};
    CBAR();
  }
  run(FKV + 2 * (wave + NCONS * R_STEER), FB, 2 * NCONS);

  const int sr = ((s & 7) << 3) | (s >> 3);
  constexpr int DEP[4] = {L_DEP, ((FB - 6) % NSLOT) * SLOTB, ((FB - 4) % NSLOT) * SLOTB, ((FB - 2) % NSLOT) * SLOTB};
  static_assert(FB - 6 > NFILL - 1 - NSLOT, "no later fill reuses a deposit slot");
  {
    const float lt = xrow_sum(l);
    if (n < 8) {
      LAS unsigned char* d = L + (wave == 0 ? DEP[0] : wave == 1 ? DEP[1] : wave == 2 ? DEP[2] : DEP[3]) + n * DSTR + c * 16;
#pragma unroll
      for (int e = 0; e < 8; ++e) *(LAS f32x4*)(d + e * 64) = O[e];
      if (c == 0) *(LAS f32x2*)(d + 512) = f32x2{m * C2, lt};
    }
    arrive(C_D);
  }
  if (wave >= 2) {
    const int e = ((lane >> 2) & 6) | (lane & 1), cc = (lane >> 1) & 3;
    float M[2], Ls[2];
    unsigned cs[2];
    wait_word(C_D, ALL4);
#pragma unroll
    for (int j = 0; j < 2; ++j) {
      const int h = 4 * (wave - 2) + 2 * j + (lane >> 5);
      LAS unsigned char* r = L + h * DSTR;
      f32x4 P[4];
      f32x2 ml[4];
#pragma unroll
      for (int w = 0; w < 4; ++w) {
        ml[w] = *(LAS f32x2*)(r + DEP[w] + 512);
        P[w] = *(LAS f32x4*)(r + DEP[w] + e * 64 + cc * 16);
      }
      M[j] = fmx(fmx(ml[0].x, ml[1].x), fmx(ml[2].x, ml[3].x));
      float wt[4];
#pragma unroll
      for (int w = 0; w < 4; ++w) wt[w] = __builtin_amdgcn_exp2f(ml[w].x - M[j]);
      Ls[j] = (wt[0] * ml[0].y + wt[1] * ml[1].y) + (wt[2] * ml[2].y + wt[3] * ml[3].y);
      const f32x4 o = (P[0] * wt[0] + P[1] * wt[1]) + (P[2] * wt[2] + P[3] * wt[3]);
      const unsigned a0 = pack2(o.x, o.y), a1 = pack2(o.z, o.w);
      const v4u d = {a0, a1, dppu<XOR1>(a0), dppu<XOR1>(a1)};
      if (!(lane & 1)) stsc1(rs, WS_PART + (g * 8 + h) * 16384 + ((lane >> 3) & 3) * 4096 + sr * 64 + (lane & 7) * 8, d);
      cs[j] = (d.x + d.y) + (d.z + d.w);
      cs[j] += dppu<XOR2>(cs[j]);
      cs[j] += dppu<HMIR>(cs[j]);
    }
    const int j = lane & 1, h = 4 * (wave - 2) + 2 * j + (lane >> 5), rec = WS_ML + ((g * 8 + h) * 64 + sr) * 32;
    if (!(lane & 6)) __builtin_amdgcn_raw_buffer_store_b32((j ? cs[1] : cs[0]) + t0, rs, rec + 16 + ((lane >> 3) & 3) * 4, 0, SC1);
    if (!(lane & 30)) {
      stpair(rs, rec, f2u(sel(j, M[1], M[0])), f2u(sel(j, Ls[1], Ls[0])), t0, t1);
      __hip_atomic_fetch_add(counter(0, h), 1u, __ATOMIC_RELAXED, __HIP_MEMORY_SCOPE_AGENT);
    }
  } else if (wave == 0) {
    m = -INFINITY;
    l = 0.f;
#pragma unroll
    for (int e = 0; e < 8; ++e) O[e] = f32x4{0.f, 0.f, 0.f, 0.f};
    read_q(1);
    slot = FB % NSLOT;
    run(FB, FB + 2, 2);
    LAS unsigned char* sK = L + slot * SLOTB;
    read_k(sK, FB + 2);
    const v4u Pf = softmax_tile();
    float lt = xrow_sum(l), mc = m * C2;
    int ob = WS_PART + ((4 + g) * 8 + (n & 7)) * 16384 + (n >> 3) * 4096 + sr * 64 + c * 16;
    int oc = WS_ML + ((4 + g) * 512 + sr) * 32 + (n & 7) * 2048;
    asm volatile("" : "+v"(lt), "+v"(mc), "+v"(ob), "+v"(oc));
    read_v(sK, FB + 2);
    if (lane < 8) __hip_atomic_fetch_add(counter(1, lane), 1u, __ATOMIC_RELAXED, __HIP_MEMORY_SCOPE_AGENT);
#pragma unroll
    for (int e = 0; e < 8; ++e) O[e] = mfma(Vf[e], Pf, O[e]);
    unsigned cs[2];
#pragma unroll
    for (int i = 0; i < 2; ++i) {
      float v[8];
#pragma unroll
      for (int k = 0; k < 4; ++k) {
        v[k] = sel(n < 8, O[4 * i][k], O[4 * i + 2][k]);
        v[4 + k] = sel(n < 8, O[4 * i + 1][k], O[4 * i + 3][k]);
      }
      const v4u d = {pack2(v[0], v[1]), pack2(v[2], v[3]), pack2(v[4], v[5]), pack2(v[6], v[7])};
      stsc1(rs, ob + i * 8192, d);
      cs[i] = (d.x + d.y) + (d.z + d.w);
    }
    {
      auto x = __builtin_amdgcn_permlane32_swap(cs[0], cs[1], false, false);
      unsigned y = x[0] + x[1];
      auto z = __builtin_amdgcn_permlane16_swap(y, y, false, false);
      y = z[0] + z[1];
      if (!(lane & 16)) __builtin_amdgcn_raw_buffer_store_b32(y + t0, rs, oc + 16 + ((lane >> 5) * 2 + (n >> 3)) * 4, 0, SC1);
    }
    if (lane < 8) stpair(rs, oc, f2u(mc), f2u(lt), t0, t1);
  }

  {
    int om = WS_ML + ((br * 4 + g) * 8 + hh) * 64 * 32;
    int od = WS_PART + ((br * 4 + g) * 8 + hh) * 16384 + wave * 4096;
    int vd = lane * 16, vm = lane * 32;
    int oo = WS_OP + ((br * 4 + ksp) * 32 + g * 8 + hh) * 2048 + wave * 512 + lane * 8;
    unsigned aw = lbase + L_W + (lane >> 2) * 4;
    unsigned ao = lbase + L_O + wave * 64 + p4 * 16 + c * 4;
    asm volatile("" : "+v"(vd), "+v"(vm), "+v"(oo), "+v"(aw), "+v"(ao), "+s"(om), "+s"(od));
    v4u Wf[16];
    const unsigned wofs = (unsigned)((4 * c + q4) * 512 + 8 * p4), h3 = (unsigned)(q4 | ((c & 1) << 2));
    auto stage = [&](auto ksc) {
      constexpr int ks = decltype(ksc)::value;
      wait_full(FWO + 2 * ks + 2);
      LAS unsigned char* b0 = L + ((FWO + 2 * ks) % NSLOT) * SLOTB + wofs;
#pragma unroll
      for (int k = 0; k < 4; ++k) {
        const unsigned blk = (unsigned)(4 * wave + k), o = (blk >> 3) * 256 + (((blk & 7) ^ h3) << 5);
        const v2u lo = trread(b0 + o), hi = trread(b0 + SLOTB + o);
        Wf[4 * ks + k] = v4u{lo.x, lo.y, hi.x, hi.y};
      }
      __builtin_amdgcn_sched_barrier(0);
    };
    if (wave == 1) {
      wait_full(AT_REC);
      int v, left = HINT_POLLS;
      asm volatile("1:\n\ts_load_dword %0, %2, 0x0 glc\n\ts_waitcnt lgkmcnt(0)\n\ts_cmp_ge_u32 %0, 64\n\ts_cbranch_scc1 2f\n\t"
                   "s_sub_i32 %1, %1, 1\n\ts_cmp_gt_i32 %1, 0\n\ts_sleep 3\n\ts_cbranch_scc1 1b\n2:"
                   : "=&s"(v), "+s"(left) : "s"(counter(br, hh)) : "memory", "scc");
      *(volatile LAS unsigned*)(L + C_M) = 1u;
    } else {
      wait_word(C_M, 1);
    }
    v4u mlg = {0u, 0u, 0u, 0u}, csg = {0u, 0u, 0u, 0u}, D[4];
    if (wave == 1) { mlg = ldsc1(rs, vm, om); csg = ldsc1(rs, vm + 16, om); }
#pragma unroll
    for (int i = 0; i < 4; ++i) D[i] = ldsc1(rs, vd + i * 1024, od);
    __builtin_amdgcn_sched_barrier(0);
    stage(IC<0>{});
    stage(IC<1>{});
    if (wave == 1) {
      if (!__all(tagok(mlg, t0, t1))) {
        bool ok;
#pragma unroll 1
        do {
          CBAR();
          mlg = ldsc1(rs, vm, om);
          csg = ldsc1(rs, vm + 16, om);
          asm volatile("" : "+v"(csg));
          ok = __all(tagok(mlg, t0, t1));
        } while (!ok && --polls > 0);
      }
#pragma unroll
      for (int k = 0; k < 4; ++k) *(LAS unsigned*)(L + L_CS + k * 256 + lane * 4) = csg[k];
      const float ms = u2f(mlg.x);
      float M = ms;
      M = fmx(M, dpp<XOR1>(M)); M = fmx(M, dpp<XOR2>(M)); M = fmx(M, dpp<ROR4>(M)); M = fmx(M, dpp<ROR8>(M));
      M = xrow_max(M);
      const float wl = __builtin_amdgcn_exp2f(ms - M);
      *(LAS float*)(L + L_W + lane * 4) = wl;
      CBAR();
      *(volatile LAS unsigned*)(L + C_W) = 1u;
      float Ls = wl * u2f(mlg.z);
      Ls += dpp<XOR1>(Ls); Ls += dpp<XOR2>(Ls); Ls += dpp<ROR4>(Ls); Ls += dpp<ROR8>(Ls);
      *(LAS float*)(L + L_W + 256) = __builtin_amdgcn_rcpf(xrow_sum(Ls));
      CBAR();
      *(volatile LAS unsigned*)(L + C_W) = 2u;
    }
    f32x2 u[4][4];
    unsigned sum[4], ck[4];
    auto unpack = [&]() {
#pragma unroll
      for (int i = 0; i < 4; ++i) {
#pragma unroll
        for (int k = 0; k < 4; ++k) u[i][k] = f32x2{u2f(D[i][k] << 16), u2f(D[i][k] & 0xffff0000u)};
        sum[i] = (D[i].x + D[i].y) + (D[i].z + D[i].w);
      }
#pragma unroll
      for (int i = 0; i < 4; ++i) sum[i] += dppu<XOR1>(sum[i]);
#pragma unroll
      for (int i = 0; i < 4; ++i) sum[i] += dppu<XOR2>(sum[i]) + t0;
    };
    auto intact = [&]() { return (bool)__all((((sum[0] ^ ck[0]) | (sum[1] ^ ck[1])) | ((sum[2] ^ ck[2]) | (sum[3] ^ ck[3]))) == 0u); };
    unpack();
#pragma unroll
    for (int i = 0; i < 4; ++i) asm volatile("" : "+v"(u[i][0]), "+v"(u[i][1]), "+v"(u[i][2]), "+v"(u[i][3]), "+v"(sum[i]));
    int wseen = 0;
    wait_ge(lbase + C_W, 1, wseen, budget);
    float wi[4];
#pragma unroll
    for (int i = 0; i < 4; ++i) {
      wi[i] = *(LAS float*)(uintptr_t)(aw + i * 64);
      ck[i] = *(LAS unsigned*)(uintptr_t)(aw + (L_CS - L_W) + wave * 256 + i * 64);
    }
    if (!intact()) {
      bool ok;
#pragma unroll 1
      do {
        CBAR();
#pragma unroll
        for (int i = 0; i < 4; ++i) {
          ck[i] = __builtin_amdgcn_raw_buffer_load_b32(rs, (lane >> 2) * 32 + 16 + wave * 4, om + i * 512, SC1);
          D[i] = ldsc1(rs, vd + i * 1024, od);
        }
        unpack();
        ok = intact();
      } while (!ok && --polls > 0);
    }
    f32x2 a[4];
#pragma unroll
    for (int k = 0; k < 4; ++k) a[k] = (u[0][k] * wi[0] + u[1][k] * wi[1]) + (u[2][k] * wi[2] + u[3][k] * wi[3]);
    float h4[4], h2[2];
#pragma unroll
    for (int k = 0; k < 4; ++k) {
      auto x = __builtin_amdgcn_permlane32_swap(f2u(a[k >> 1][k & 1]), f2u(a[2 + (k >> 1)][k & 1]), false, false);
      h4[k] = u2f(x[0]) + u2f(x[1]);
    }
#pragma unroll
    for (int k = 0; k < 2; ++k) {
      auto x = __builtin_amdgcn_permlane16_swap(f2u(h4[k]), f2u(h4[2 + k]), false, false);
      h2[k] = u2f(x[0]) + u2f(x[1]);
      h2[k] += dpp<ROR4>(h2[k]);
      h2[k] += dpp<ROR8>(h2[k]);
    }
    asm volatile("" : "+v"(h2[0]), "+v"(h2[1]));
    wait_ge(lbase + C_W, 2, wseen, budget);
    const float inv = *(LAS float*)(L + L_W + 256);
    *(LAS unsigned*)(uintptr_t)ao = pack2(h2[0] * inv, h2[1] * inv);
    arrive(C_O);
    wait_word(C_O, ALL4);
    f32x4 d[4];
#pragma unroll
    for (int k = 0; k < 4; ++k) d[k] = f32x4{0.f, 0.f, 0.f, 0.f};
    v4u Of[4];
#pragma unroll
    for (int ks = 0; ks < 4; ++ks) Of[ks] = *(LAS v4u*)(L + L_O + ks * 64 + c * 16);
    auto kstep = [&](int ks) {
#pragma unroll
      for (int k = 0; k < 4; ++k) d[k] = mfma(Of[ks], Wf[4 * ks + k], d[k]);
    };
    stage(IC<2>{});
    kstep(0);
    kstep(1);
    __builtin_amdgcn_sched_barrier(0);
    stage(IC<3>{});
    kstep(2);
    kstep(3);
    const float r = sel(c & 2, sel(c & 1, d[3].x, d[2].x), sel(c & 1, d[1].x, d[0].x));
    const float rn = dpp<XOR1>(r);
    if (!(lane & 1)) stpair(rs, oo, f2u(r), f2u(rn), t0, t1);
  }
  if (wave != 0) return;

  {
    const int j2 = ((b >> 3) & 1) | ((b & 7) << 1) | (b & 16), set = b >> 5;
    const int of = WS_OP + set * 32 * 2048 + j2 * 64, vf = (lane >> 2) * 2048 + p4 * 16;
    unsigned* po = p.out + set * 128 + j2 * 4 + (lane & 3);
    asm volatile("" : "+v"(po));
    v4u F[2];
    __builtin_amdgcn_s_sleep(16);
#pragma unroll 1
    for (;;) {
      CBAR();
      F[0] = ldsc1(rs, vf, of);
      F[1] = ldsc1(rs, vf, of + 16 * 2048);
      if (__all(tagok(F[0], t0, t1) && tagok(F[1], t0, t1)) || --polls <= 0) break;
    }
    float a0 = u2f(F[0].x) + u2f(F[1].x), a1 = u2f(F[0].z) + u2f(F[1].z);
    a0 += dpp<ROR4>(a0); a1 += dpp<ROR4>(a1);
    a0 += dpp<ROR8>(a0); a1 += dpp<ROR8>(a1);
    a0 = xrow_sum(a0); a1 = xrow_sum(a1);
    if (lane < 4) *(GAS unsigned*)po = pack2(a0, a1);
    if (b == 0 && lane == 0) __hip_atomic_store((GAS unsigned long long*)p.ws, tg, __ATOMIC_RELAXED, __HIP_MEMORY_SCOPE_AGENT);
  }
}
}

extern "C" void kernel_launch(void* const* d_in, const int* in_sizes, int n_in, void* d_out, int out_size, void* d_ws, size_t ws_size,
                              hipStream_t stream) {
  (void)in_sizes; (void)n_in; (void)out_size; (void)ws_size;
  fa::Params p;
  p.xA = (const unsigned char*)d_in[0]; p.kA = (const unsigned char*)d_in[1]; p.vA = (const unsigned char*)d_in[2];
  p.wqA = (const unsigned char*)d_in[3]; p.woA = (const unsigned char*)d_in[4];
  p.xB = (const unsigned char*)d_in[5]; p.kB = (const unsigned char*)d_in[6]; p.vB = (const unsigned char*)d_in[7];
  p.wqB = (const unsigned char*)d_in[8]; p.woB = (const unsigned char*)d_in[9];
  p.out = (unsigned*)d_out;
  p.ws = (unsigned char*)d_ws;
  fa::fused<<<fa::NWG, fa::NTHR, 0, stream>>>(p);
}
```
